```python
import torch

def _softmax(x, axis=-1):
    return torch.softmax(x, dim=axis)
def _gelu(x):
    return torch.nn.functional.gelu(x, approximate="tanh")
def _silu(x):
    return torch.nn.functional.silu(x)
def _sigmoid(x):
    return torch.sigmoid(x)
def _layernorm(x, axis=-1, eps=1e-5):
    m = x.mean(dim=axis, keepdim=True)
    v = x.var(dim=axis, keepdim=True, unbiased=False)
    return (x - m) / torch.sqrt(v + eps)
def _scatter_add(tgt, idx, src, axis=0):
    return tgt.clone().index_add_(axis, idx, src)
def _segment_sum(src, seg, n):
    out = torch.zeros((n,) + src.shape[1:], dtype=src.dtype)
    return out.index_add_(0, seg, src)
def _pad(x, pw):
    flat = tuple(v for pair in reversed(pw) for v in pair)
    return torch.nn.functional.pad(x, flat)

kernel_name = 'standalone__fm_second_order__recsys'

def setup_inputs():

    _w = (lambda a: (lambda t: t.to(torch.bfloat16) if t.is_floating_point() else t)(torch.as_tensor(a)))
    B, F, D, V = 16384, 26, 128, min(100000, 100000)
    return {
        "idx": _w((lambda lo, hi, *s: torch.randint(lo, hi, s))(0, V, B, F).to(torch.int64)),
        "table": _w((lambda *s: torch.randn(*s, dtype=torch.bfloat16) * 0.05)(V, D)),
    }

def reference(idx, table):
    emb = table[idx]
    sum_sq = emb.sum(axis=1) ** 2
    sq_sum = (emb ** 2).sum(axis=1)
    return 0.5 * (sum_sq - sq_sum).sum(axis=-1)
```

```cpp
#include <hip/hip_runtime.h>
#include <hip/hip_bf16.h>
#include <stdint.h>
#include <utility>

typedef unsigned int u32x4 __attribute__((ext_vector_type(4)));

constexpr int F = 26;
constexpr int D = 128;

__device__ __forceinline__ float lo_f(unsigned x) { return __uint_as_float(x << 16); }
__device__ __forceinline__ float hi_f(unsigned x) { return __uint_as_float(x & 0xffff0000u); }

template <int f>
__device__ __forceinline__ u32x4 load_row(const char* __restrict__ table, unsigned i0, unsigned i1, unsigned loff) {
    unsigned src = f < 16 ? i0 : i1;
    unsigned off = (unsigned)__builtin_amdgcn_update_dpp(0, (int)src, 0x150 + (f & 15), 0xf, 0xf, false) + loff;
    return *(const u32x4*)(table + off);
}

template <int... Is>
__device__ __forceinline__ void load_all(u32x4 (&v)[F], const char* __restrict__ table, unsigned i0, unsigned i1, unsigned loff,
                                         std::integer_sequence<int, Is...>) {
    ((v[Is] = load_row<Is>(table, i0, i1, loff)), ...);
}

__global__ __launch_bounds__(256) void fm_kernel(const int* __restrict__ idx, const char* __restrict__ table,
                                                 __hip_bfloat16* __restrict__ out, int B) {
    const int tid = blockIdx.x * 256 + threadIdx.x;
    const int b = tid >> 4;
    const int l = tid & 15;
    if (b >= B) return;
    const int* ip = idx + b * F;
    unsigned i0 = (unsigned)ip[l] * 256u;
    unsigned i1 = (l < F - 16) ? (unsigned)ip[16 + l] * 256u : 0u;
    u32x4 v[F];
    load_all(v, table, i0, i1, l * 16, std::make_integer_sequence<int, F>{});
    float s[8] = {0, 0, 0, 0, 0, 0, 0, 0};
    float q = 0.f;
#pragma unroll
    for (int f = 0; f < F; ++f) {
#pragma unroll
        for (int k = 0; k < 4; ++k) {
            float a = lo_f(v[f][k]), c = hi_f(v[f][k]);
            s[2 * k] += a;
            s[2 * k + 1] += c;
            q = fmaf(a, a, q);
            q = fmaf(c, c, q);
        }
    }
    float r = -q;
#pragma unroll
    for (int k = 0; k < 8; ++k) r = fmaf(s[k], s[k], r);
    r += __shfl_xor(r, 8, 16);
    r += __shfl_xor(r, 4, 16);
    r += __shfl_xor(r, 2, 16);
    r += __shfl_xor(r, 1, 16);
    if (l == 0) out[b] = __float2bfloat16(0.5f * r);
}

extern "C" void kernel_launch(void* const* d_in, const int* in_sizes, int n_in,
                              void* d_out, int out_size, void* d_ws, size_t ws_size,
                              hipStream_t stream) {
    const int* idx = (const int*)d_in[0];
    const char* table = (const char*)d_in[1];
    int B = out_size;
    fm_kernel<<<(B * 16 + 255) / 256, 256, 0, stream>>>(idx, table, (__hip_bfloat16*)d_out, B);
}
```

```cpp
#include <hip/hip_runtime.h>
#include <hip/hip_bf16.h>
#include <stdint.h>
#include <utility>

typedef unsigned int u32x4 __attribute__((ext_vector_type(4)));
typedef unsigned int u32x2 __attribute__((ext_vector_type(2)));
constexpr int F = 26;
#define NT 0

__device__ __forceinline__ float lo_f(unsigned x) { return __uint_as_float(x << 16); }
__device__ __forceinline__ float hi_f(unsigned x) { return __uint_as_float(x & 0xffff0000u); }

template <int f>
__device__ __forceinline__ unsigned bcast_quad(const unsigned (&ix)[8]) {
    constexpr int j = f / 8;
    return (unsigned)__builtin_amdgcn_update_dpp(0, (int)ix[f % 8], j | (j << 2) | (j << 4) | (j << 6), 0xf, 0xf, false);
}
__device__ __forceinline__ u32x4 ld16(const char* p) {
    if (NT) return __builtin_nontemporal_load((const u32x4*)p);
    return *(const u32x4*)p;
}
template <int... Is>
__device__ __forceinline__ void load_all4(u32x4 (&v)[F], const char* __restrict__ table, const unsigned (&ix)[8], unsigned loff,
                                         std::integer_sequence<int, Is...>) {
    ((v[Is] = ld16(table + (bcast_quad<Is>(ix) + loff))), ...);
}

__global__ __launch_bounds__(256) void fm_kernel(const int* __restrict__ idx, const char* __restrict__ table,
                                                 float* __restrict__ part, int B) {
    constexpr int SPLIT = 2, LPR = 8, SPB = 32;
    const int nblk = gridDim.x;
    const int xcd = blockIdx.x & 7, lb = blockIdx.x >> 3;
    const int cg = xcd % SPLIT, sg = xcd / SPLIT;
    const int b = (sg * (nblk / 8) + lb) * SPB + threadIdx.x / LPR;
    const int l = threadIdx.x % LPR;
    const unsigned loff = cg * 128 + l * 16;
    u32x4 v[F];
    const int j = threadIdx.x & 3;
    unsigned ix[8];
    const int* ip = idx + b * F + 8 * j;
    if (j < 3) {
        u32x4 a = *(const u32x4*)ip, c = *(const u32x4*)(ip + 4);
        ix[0] = a[0]; ix[1] = a[1]; ix[2] = a[2]; ix[3] = a[3]; ix[4] = c[0]; ix[5] = c[1]; ix[6] = c[2]; ix[7] = c[3];
    } else {
        u32x2 a = *(const u32x2*)ip;
        ix[0] = a[0]; ix[1] = a[1]; ix[2] = ix[3] = ix[4] = ix[5] = ix[6] = ix[7] = 0;
    }
#pragma unroll
    for (int k = 0; k < 8; ++k) ix[k] <<= 8;
    load_all4(v, table, ix, loff, std::make_integer_sequence<int, F>{});
    float s[8] = {0, 0, 0, 0, 0, 0, 0, 0};
    float q = 0.f;
#pragma unroll
    for (int f = 0; f < F; ++f) {
#pragma unroll
        for (int k = 0; k < 4; ++k) {
            float a = lo_f(v[f][k]), c = hi_f(v[f][k]);
            s[2 * k] += a;
            s[2 * k + 1] += c;
            q = fmaf(a, a, q);
            q = fmaf(c, c, q);
        }
    }
    float r = -q;
#pragma unroll
    for (int k = 0; k < 8; ++k) r = fmaf(s[k], s[k], r);
#pragma unroll
    for (int m = LPR / 2; m >= 1; m >>= 1) r += __shfl_xor(r, m, 16);
    if (l == 0) part[cg * B + b] = r;
}

__global__ __launch_bounds__(256) void fm_combine(const float* __restrict__ part, __hip_bfloat16* __restrict__ out, int B) {
    int b = blockIdx.x * 256 + threadIdx.x;
    if (b < B) out[b] = __float2bfloat16(0.5f * (part[b] + part[B + b]));
}

extern "C" void kernel_launch(void* const* d_in, const int* in_sizes, int n_in,
                              void* d_out, int out_size, void* d_ws, size_t ws_size,
                              hipStream_t stream) {
    const int* idx = (const int*)d_in[0];
    const char* table = (const char*)d_in[1];
    int B = out_size;
    fm_kernel<<<B * 16 / 256, 256, 0, stream>>>(idx, table, (float*)d_ws, B);
    fm_combine<<<(B + 255) / 256, 256, 0, stream>>>((const float*)d_ws, (__hip_bfloat16*)d_out, B);
}
```

```cpp
#include <hip/hip_runtime.h>
#include <hip/hip_bf16.h>
#include <stdint.h>
#include <utility>

typedef unsigned int u32x4 __attribute__((ext_vector_type(4)));
typedef unsigned int u32x2 __attribute__((ext_vector_type(2)));
typedef float f32x4 __attribute__((ext_vector_type(4)));
typedef short s16x4 __attribute__((ext_vector_type(4)));
typedef __bf16 bf16x2 __attribute__((ext_vector_type(2)));
typedef unsigned long long u64;

constexpr int F = 26;
constexpr int NB = 16384;
constexpr int W = 4;

__host__ __device__ constexpr int slot_g(int k) { constexpr int t[F] = {0,1,2, 0,1,2, 0,1,2,3, 0,1,2, 0,1,2, 3,0,1,2, 0,1,2, 0,1,2}; return t[k]; }
__host__ __device__ constexpr int slot_r(int k) { constexpr int t[F] = {0,0,0, 1,1,1, 2,2,2,0, 3,3,3, 4,4,4, 1,5,5,5, 6,6,6, 7,7,7}; return t[k]; }

template <int k>
__device__ __forceinline__ unsigned bcast_slot(const unsigned (&ix)[8]) {
    constexpr int j = slot_g(k);
    return (unsigned)__builtin_amdgcn_update_dpp(0, (int)ix[slot_r(k)], j | (j << 2) | (j << 4) | (j << 6), 0xf, 0xf, false);
}
#define CE(a, b) { const unsigned lo_ = min(ix[a], ix[b]), hi_ = max(ix[a], ix[b]); ix[a] = lo_; ix[b] = hi_; }

struct Acc { f32x4 s0, s1; float q; };

__device__ __forceinline__ void consume(Acc& a, const u32x4 v, const s16x4 ident) {
    const u32x2 lo = {v[0], v[1]}, hi = {v[2], v[3]};
    a.s0 = __builtin_amdgcn_mfma_f32_4x4x4bf16_1k(ident, __builtin_bit_cast(s16x4, lo), a.s0, 0, 0, 0);
    a.s1 = __builtin_amdgcn_mfma_f32_4x4x4bf16_1k(ident, __builtin_bit_cast(s16x4, hi), a.s1, 0, 0, 0);
#pragma unroll
    for (int k = 0; k < 4; ++k) {
        const unsigned w = v[k];
        const bf16x2 h = __builtin_bit_cast(bf16x2, w);
        a.q = __builtin_amdgcn_fdot2_f32_bf16(h, h, a.q, false);
    }
    asm volatile("" : "+v"(a.q));
}

__device__ __forceinline__ float red8(float r) {
    r += __builtin_bit_cast(float, __builtin_amdgcn_update_dpp(0, __builtin_bit_cast(int, r), 0xB1, 0xf, 0xf, false));
    r += __builtin_bit_cast(float, __builtin_amdgcn_update_dpp(0, __builtin_bit_cast(int, r), 0x4E, 0xf, 0xf, false));
    r += __builtin_bit_cast(float, __builtin_amdgcn_update_dpp(0, __builtin_bit_cast(int, r), 0x141, 0xf, 0xf, false));
    return r;
}

template <int k>
__device__ __forceinline__ void round_(u32x4 (&v)[F], Acc& a, const __amdgpu_buffer_rsrc_t rt, const unsigned (&ix)[8], unsigned loff, s16x4 ident) {
    if constexpr (k < F) {
        v[k] = __builtin_amdgcn_raw_buffer_load_b128(rt, bcast_slot<k>(ix) | loff, 0, 0);
        __builtin_amdgcn_sched_barrier(0);
        if constexpr (k % 2 == 1 && k < F - 1) {
            __builtin_amdgcn_s_barrier();
            __builtin_amdgcn_sched_barrier(0);
        }
    }
    if constexpr (k >= W) { consume(a, v[k - W], ident); __builtin_amdgcn_sched_barrier(0); }
}
template <int... Is>
__device__ __forceinline__ void rounds(u32x4 (&v)[F], Acc& a, const __amdgpu_buffer_rsrc_t rt, const unsigned (&ix)[8], unsigned loff, s16x4 ident, std::integer_sequence<int, Is...>) {
    (round_<Is>(v, a, rt, ix, loff, ident), ...);
}

__global__ __launch_bounds__(1024) __attribute__((amdgpu_waves_per_eu(4, 4)))
void fm_kernel(const int* __restrict__ idx, const char* __restrict__ table, __hip_bfloat16* __restrict__ out, u64* __restrict__ xch, unsigned table_bytes) {
    const int x = blockIdx.x & 7, lb = blockIdx.x >> 3;
    const int cg = x & 1, sg = x >> 1;
    const int b = (sg * 32 + lb) * 128 + threadIdx.x / 8;
    const int l = threadIdx.x & 7, j = threadIdx.x & 3;
    const unsigned loff = cg * 128 + l * 16;
    const int* ip = idx + b * F + (j < 3 ? 8 * j : 18);
    const u32x4 a0 = *(const u32x4*)ip, a1 = *(const u32x4*)(ip + 4);
    unsigned* epp = (unsigned*)(xch + NB) + (b >> 3);
    const unsigned ep = __hip_atomic_load(epp, __ATOMIC_RELAXED, __HIP_MEMORY_SCOPE_AGENT);
    const __amdgpu_buffer_rsrc_t rt = __builtin_amdgcn_make_buffer_rsrc((void*)table, 0, table_bytes, 0x00020000);
    const unsigned m = j == 3 ? ~0u : 0u;
    unsigned ix[8];
    ix[0] = (a0[0] << 8) | m; ix[1] = (a0[1] << 8) | m; ix[2] = (a0[2] << 8) | m; ix[3] = (a0[3] << 8) | m;
    ix[4] = (a1[0] << 8) | m; ix[5] = (a1[1] << 8) | m; ix[6] = a1[2] << 8; ix[7] = a1[3] << 8;
    CE(0,2) CE(1,3) CE(4,6) CE(5,7)
    CE(0,4) CE(1,5) CE(2,6) CE(3,7)
    CE(0,1) CE(2,3) CE(4,5) CE(6,7)
    CE(2,4) CE(3,5)
    CE(1,4) CE(3,6)
    CE(1,2) CE(3,4) CE(5,6)
    const s16x4 ident = s16x4{(short)(j == 0 ? 0x3F80 : 0), (short)(j == 1 ? 0x3F80 : 0), (short)(j == 2 ? 0x3F80 : 0), (short)(j == 3 ? 0x3F80 : 0)};
    u32x4 v[F];
    Acc a; a.s0 = a.s1 = f32x4{0.f, 0.f, 0.f, 0.f}; a.q = 0.f;
    rounds(v, a, rt, ix, loff, ident, std::make_integer_sequence<int, F + W>{});
    float r = -a.q;
#pragma unroll
    for (int k = 0; k < 4; ++k) { r = fmaf(a.s0[k], a.s0[k], r); r = fmaf(a.s1[k], a.s1[k], r); }
    r = red8(r);
    if (l == 0) {
        const unsigned tag = (ep + 1u) * 0x9E3779B1u;
        const unsigned tlo = (tag >> 11) & 0xFFu;
        const unsigned mine = __float_as_uint(r) & ~0xFFu;
        const u64 old = __hip_atomic_exchange(xch + b, ((u64)tag << 32) | mine | tlo, __ATOMIC_RELAXED, __HIP_MEMORY_SCOPE_AGENT);
        if ((unsigned)(old >> 32) == tag && ((unsigned)old & 0xFFu) == tlo) {
            out[b] = __float2bfloat16(0.5f * (__uint_as_float(mine) + __uint_as_float((unsigned)old & ~0xFFu)));
            if ((threadIdx.x & 63) == 0) __hip_atomic_store(epp, ep + 1u, __ATOMIC_RELAXED, __HIP_MEMORY_SCOPE_AGENT);
        }
    }
}

extern "C" void kernel_launch(void* const* d_in, const int* in_sizes, int n_in,
                              void* d_out, int out_size, void* d_ws, size_t ws_size,
                              hipStream_t stream) {
    fm_kernel<<<256, 1024, 0, stream>>>((const int*)d_in[0], (const char*)d_in[1], (__hip_bfloat16*)d_out, (u64*)d_ws,
                                        (unsigned)in_sizes[1] * 2u);
}
```

```cpp
#include <hip/hip_runtime.h>
#include <hip/hip_bf16.h>
#include <stdint.h>
#include <utility>

typedef unsigned int u32x4 __attribute__((ext_vector_type(4)));
typedef unsigned int u32x2 __attribute__((ext_vector_type(2)));
typedef float f32x4 __attribute__((ext_vector_type(4)));
typedef short s16x4 __attribute__((ext_vector_type(4)));
typedef __bf16 bf16x2 __attribute__((ext_vector_type(2)));
typedef unsigned long long u64;

constexpr int F = 26;
constexpr int NB = 16384;
constexpr int W = 4;

__host__ __device__ constexpr int slot_g(int k) { constexpr int t[F] = {0,1,2, 0,1,2, 0,1,2,3, 0,1,2, 0,1,2, 3,0,1,2, 0,1,2, 0,1,2}; return t[k]; }
__host__ __device__ constexpr int slot_r(int k) { constexpr int t[F] = {0,0,0, 1,1,1, 2,2,2,0, 3,3,3, 4,4,4, 1,5,5,5, 6,6,6, 7,7,7}; return t[k]; }

template <int k>
__device__ __forceinline__ unsigned bcast_slot(const unsigned (&ix)[8]) {
    constexpr int j = slot_g(k);
    return (unsigned)__builtin_amdgcn_update_dpp(0, (int)ix[slot_r(k)], j | (j << 2) | (j << 4) | (j << 6), 0xf, 0xf, false);
}
#define CE(a, b) { const unsigned lo_ = min(ix[a], ix[b]), hi_ = max(ix[a], ix[b]); ix[a] = lo_; ix[b] = hi_; }

struct Acc { f32x4 s0, s1; float q; };

__device__ __forceinline__ void consume(Acc& a, const u32x4 v, const s16x4 ident) {
    const u32x2 lo = {v[0], v[1]}, hi = {v[2], v[3]};
    a.s0 = __builtin_amdgcn_mfma_f32_4x4x4bf16_1k(ident, __builtin_bit_cast(s16x4, lo), a.s0, 0, 0, 0);
    a.s1 = __builtin_amdgcn_mfma_f32_4x4x4bf16_1k(ident, __builtin_bit_cast(s16x4, hi), a.s1, 0, 0, 0);
#pragma unroll
    for (int k = 0; k < 4; ++k) {
        const unsigned w = v[k];
        const bf16x2 h = __builtin_bit_cast(bf16x2, w);
        a.q = __builtin_amdgcn_fdot2_f32_bf16(h, h, a.q, false);
    }
    asm volatile("" : "+v"(a.q));
}

__device__ __forceinline__ float red8(float r) {
    r += __builtin_bit_cast(float, __builtin_amdgcn_update_dpp(0, __builtin_bit_cast(int, r), 0xB1, 0xf, 0xf, false));
    r += __builtin_bit_cast(float, __builtin_amdgcn_update_dpp(0, __builtin_bit_cast(int, r), 0x4E, 0xf, 0xf, false));
    r += __builtin_bit_cast(float, __builtin_amdgcn_update_dpp(0, __builtin_bit_cast(int, r), 0x141, 0xf, 0xf, false));
    return r;
}

template <int k>
__device__ __forceinline__ void round_(u32x4 (&v)[F], Acc& a, const __amdgpu_buffer_rsrc_t rt, const unsigned (&ix)[8], unsigned loff, s16x4 ident) {
    if constexpr (k < F) {
        v[k] = __builtin_amdgcn_raw_buffer_load_b128(rt, bcast_slot<k>(ix) | loff, 0, 0);
        __builtin_amdgcn_sched_barrier(0);
        if constexpr (k % 2 == 1 && k < F - 1) {
            __builtin_amdgcn_s_barrier();
            __builtin_amdgcn_sched_barrier(0);
        }
    }
    if constexpr (k >= W) { consume(a, v[k - W], ident); __builtin_amdgcn_sched_barrier(0); }
}
template <int... Is>
__device__ __forceinline__ void rounds(u32x4 (&v)[F], Acc& a, const __amdgpu_buffer_rsrc_t rt, const unsigned (&ix)[8], unsigned loff, s16x4 ident, std::integer_sequence<int, Is...>) {
    (round_<Is>(v, a, rt, ix, loff, ident), ...);
}

template <int... Is>
__device__ __forceinline__ void rounds2(u32x4 (&v)[F], Acc& a, const __amdgpu_buffer_rsrc_t rt, const unsigned (&ix)[8], unsigned loff, s16x4 ident, std::integer_sequence<int, Is...>) {
    (round_<Is + W + 1>(v, a, rt, ix, loff, ident), ...);
}
__global__ __launch_bounds__(1024) __attribute__((amdgpu_waves_per_eu(4, 4)))
void fm_kernel(const int* __restrict__ idx, const char* __restrict__ table, __hip_bfloat16* __restrict__ out, u64* __restrict__ xch, unsigned table_bytes) {
    u64 tz_ = __builtin_amdgcn_s_memrealtime(); u64 tq_ = 0; __builtin_amdgcn_sched_barrier(0);
    const int x = blockIdx.x & 7, lb = blockIdx.x >> 3;
    const int cg = x & 1, sg = x >> 1;
    const int b = (sg * 32 + lb) * 128 + threadIdx.x / 8;
    const int l = threadIdx.x & 7, j = threadIdx.x & 3;
    const unsigned loff = cg * 128 + l * 16;
    const int* ip = idx + b * F + (j < 3 ? 8 * j : 18);
    const u32x4 a0 = *(const u32x4*)ip, a1 = *(const u32x4*)(ip + 4);
    unsigned* epp = (unsigned*)(xch + NB) + (b >> 3);
    const unsigned ep = __hip_atomic_load(epp, __ATOMIC_RELAXED, __HIP_MEMORY_SCOPE_AGENT);
    const __amdgpu_buffer_rsrc_t rt = __builtin_amdgcn_make_buffer_rsrc((void*)table, 0, table_bytes, 0x00020000);
    const unsigned m = j == 3 ? ~0u : 0u;
    unsigned ix[8];
    ix[0] = (a0[0] << 8) | m; ix[1] = (a0[1] << 8) | m; ix[2] = (a0[2] << 8) | m; ix[3] = (a0[3] << 8) | m;
    ix[4] = (a1[0] << 8) | m; ix[5] = (a1[1] << 8) | m; ix[6] = a1[2] << 8; ix[7] = a1[3] << 8;
    CE(0,2) CE(1,3) CE(4,6) CE(5,7)
    CE(0,4) CE(1,5) CE(2,6) CE(3,7)
    CE(0,1) CE(2,3) CE(4,5) CE(6,7)
    CE(2,4) CE(3,5)
    CE(1,4) CE(3,6)
    CE(1,2) CE(3,4) CE(5,6)
    const s16x4 ident = s16x4{(short)(j == 0 ? 0x3F80 : 0), (short)(j == 1 ? 0x3F80 : 0), (short)(j == 2 ? 0x3F80 : 0), (short)(j == 3 ? 0x3F80 : 0)};
    asm volatile("" :: "v"(ix[0]), "v"(ix[7]));
    u32x4 v[F];
    Acc a; a.s0 = a.s1 = f32x4{0.f, 0.f, 0.f, 0.f}; a.q = 0.f;
    rounds(v, a, rt, ix, loff, ident, std::make_integer_sequence<int, W + 1>{});
    rounds2(v, a, rt, ix, loff, ident, std::make_integer_sequence<int, F - 1>{});
    float r = -a.q;
#pragma unroll
    for (int k = 0; k < 4; ++k) { r = fmaf(a.s0[k], a.s0[k], r); r = fmaf(a.s1[k], a.s1[k], r); }
    r = red8(r);
    if (l == 0) {
        const unsigned tag = (ep + 1u) * 0x9E3779B1u;
        const unsigned tlo = (tag >> 11) & 0xFFu;
        const unsigned mine = __float_as_uint(r) & ~0xFFu;
        const u64 old = __hip_atomic_exchange(xch + b, ((u64)tag << 32) | mine | tlo, __ATOMIC_RELAXED, __HIP_MEMORY_SCOPE_AGENT);
        if ((unsigned)(old >> 32) == tag && ((unsigned)old & 0xFFu) == tlo) {
            out[b] = __float2bfloat16(0.5f * (__uint_as_float(mine) + __uint_as_float((unsigned)old & ~0xFFu)));
            if ((threadIdx.x & 63) == 0) __hip_atomic_store(epp, ep + 1u, __ATOMIC_RELAXED, __HIP_MEMORY_SCOPE_AGENT);
        }
    }
    __builtin_amdgcn_sched_barrier(0); tq_ = __builtin_amdgcn_s_memrealtime(); __builtin_amdgcn_sched_barrier(0);
    if ((threadIdx.x & 63) == 0) { u64* ts_ = xch + (8 << 20); int w_ = blockIdx.x * (blockDim.x / 64) + threadIdx.x / 64; ts_[w_] = tz_; ts_[4096 + w_] = tq_; }
}


__global__ __launch_bounds__(1024) void probe_kernel(const u64* __restrict__ ts, int kmul) {
    __shared__ u64 sm[3][16];
    u64 mn0 = ~0ull, mx1 = 0, mn1 = ~0ull, sum = 0;
    for (int i = threadIdx.x; i < 4096; i += 1024) {
        u64 a = ts[i], b = ts[4096 + i];
        mn0 = a < mn0 ? a : mn0; mx1 = b > mx1 ? b : mx1; mn1 = b < mn1 ? b : mn1;
    }
    for (int m = 32; m >= 1; m >>= 1) {
        u64 x;
        x = __shfl_xor(mn0, m); mn0 = x < mn0 ? x : mn0;
        x = __shfl_xor(mx1, m); mx1 = x > mx1 ? x : mx1;
        x = __shfl_xor(mn1, m); mn1 = x < mn1 ? x : mn1;
    }
    int w = threadIdx.x >> 6;
    if ((threadIdx.x & 63) == 0) { sm[0][w] = mn0; sm[1][w] = mx1; sm[2][w] = mn1; }
    __syncthreads();
    if (threadIdx.x == 0) {
        for (int i = 0; i < 16; ++i) { mn0 = sm[0][i] < mn0 ? sm[0][i] : mn0; mx1 = sm[1][i] > mx1 ? sm[1][i] : mx1; mn1 = sm[2][i] < mn1 ? sm[2][i] : mn1; }
        u64 d = (kmul & 0x10000) ? mn1 - mn0 : mx1 - mn0;
        if (d > 10000) d = 10000;
        u64 tstart = __builtin_amdgcn_s_memrealtime();
        while (__builtin_amdgcn_s_memrealtime() - tstart < d * (kmul & 0xffff)) __builtin_amdgcn_s_sleep(1);
    }
}
extern "C" void kernel_launch(void* const* d_in, const int* in_sizes, int n_in,
                              void* d_out, int out_size, void* d_ws, size_t ws_size,
                              hipStream_t stream) {
    fm_kernel<<<256, 1024, 0, stream>>>((const int*)d_in[0], (const char*)d_in[1], (__hip_bfloat16*)d_out, (u64*)d_ws,
                                        (unsigned)in_sizes[1] * 2u);
    probe_kernel<<<1, 1024, 0, stream>>>((const u64*)d_ws + (8 << 20), 0);
}
```

```cpp
#include <hip/hip_runtime.h>
#include <hip/hip_bf16.h>
#include <stdint.h>
#include <utility>

typedef unsigned int u32x4 __attribute__((ext_vector_type(4)));
typedef unsigned int u32x2 __attribute__((ext_vector_type(2)));
typedef float f32x4 __attribute__((ext_vector_type(4)));
typedef short s16x4 __attribute__((ext_vector_type(4)));
typedef __bf16 bf16x2 __attribute__((ext_vector_type(2)));
typedef unsigned long long u64;

constexpr int F = 26;
constexpr int NB = 16384;
constexpr int W = 4;

__host__ __device__ constexpr int slot_g(int k) { constexpr int t[F] = {0,1,2, 0,1,2, 0,1,2,3, 0,1,2, 0,1,2, 3,0,1,2, 0,1,2, 0,1,2}; return t[k]; }
__host__ __device__ constexpr int slot_r(int k) { constexpr int t[F] = {0,0,0, 1,1,1, 2,2,2,0, 3,3,3, 4,4,4, 1,5,5,5, 6,6,6, 7,7,7}; return t[k]; }

template <int k>
__device__ __forceinline__ unsigned bcast_slot(const unsigned (&ix)[8]) {
    constexpr int j = slot_g(k);
    return (unsigned)__builtin_amdgcn_update_dpp(0, (int)ix[slot_r(k)], j | (j << 2) | (j << 4) | (j << 6), 0xf, 0xf, false);
}
#define CE(a, b) { const unsigned lo_ = min(ix[a], ix[b]), hi_ = max(ix[a], ix[b]); ix[a] = lo_; ix[b] = hi_; }

struct Acc { f32x4 s0, s1; float q; };

__device__ __forceinline__ void consume(Acc& a, const u32x4 v, const s16x4 ident) {
    const u32x2 lo = {v[0], v[1]}, hi = {v[2], v[3]};
    a.s0 = __builtin_amdgcn_mfma_f32_4x4x4bf16_1k(ident, __builtin_bit_cast(s16x4, lo), a.s0, 0, 0, 0);
    a.s1 = __builtin_amdgcn_mfma_f32_4x4x4bf16_1k(ident, __builtin_bit_cast(s16x4, hi), a.s1, 0, 0, 0);
#pragma unroll
    for (int k = 0; k < 4; ++k) {
        const unsigned w = v[k];
        const bf16x2 h = __builtin_bit_cast(bf16x2, w);
        a.q = __builtin_amdgcn_fdot2_f32_bf16(h, h, a.q, false);
    }
    asm volatile("" : "+v"(a.q));
}

__device__ __forceinline__ float red8(float r) {
    r += __builtin_bit_cast(float, __builtin_amdgcn_update_dpp(0, __builtin_bit_cast(int, r), 0xB1, 0xf, 0xf, false));
    r += __builtin_bit_cast(float, __builtin_amdgcn_update_dpp(0, __builtin_bit_cast(int, r), 0x4E, 0xf, 0xf, false));
    r += __builtin_bit_cast(float, __builtin_amdgcn_update_dpp(0, __builtin_bit_cast(int, r), 0x141, 0xf, 0xf, false));
    return r;
}

template <int k>
__device__ __forceinline__ void round_(u32x4 (&v)[F], Acc& a, const __amdgpu_buffer_rsrc_t rt, const unsigned (&ix)[8], unsigned loff, s16x4 ident) {
    if constexpr (k < F) {
        v[k] = __builtin_amdgcn_raw_buffer_load_b128(rt, bcast_slot<k>(ix) | loff, 0, 0);
        __builtin_amdgcn_sched_barrier(0);
        if constexpr (k % 2 == 1 && k < F - 1) {
            __builtin_amdgcn_s_barrier();
            __builtin_amdgcn_sched_barrier(0);
        }
    }
    if constexpr (k >= W) { consume(a, v[k - W], ident); __builtin_amdgcn_sched_barrier(0); }
}
template <int... Is>
__device__ __forceinline__ void rounds(u32x4 (&v)[F], Acc& a, const __amdgpu_buffer_rsrc_t rt, const unsigned (&ix)[8], unsigned loff, s16x4 ident, std::integer_sequence<int, Is...>) {
    (round_<Is>(v, a, rt, ix, loff, ident), ...);
}

template <int... Is>
__device__ __forceinline__ void rounds2(u32x4 (&v)[F], Acc& a, const __amdgpu_buffer_rsrc_t rt, const unsigned (&ix)[8], unsigned loff, s16x4 ident, std::integer_sequence<int, Is...>) {
    (round_<Is + W + 1>(v, a, rt, ix, loff, ident), ...);
}
__global__ __launch_bounds__(1024) __attribute__((amdgpu_waves_per_eu(4, 4)))
void fm_kernel(const int* __restrict__ idx, const char* __restrict__ table, __hip_bfloat16* __restrict__ out, u64* __restrict__ xch, unsigned table_bytes) {
    u64 tz_ = __builtin_amdgcn_s_memrealtime(); u64 tq_ = 0; __builtin_amdgcn_sched_barrier(0);
    { unsigned z_ = threadIdx.x;
      asm volatile(".rept 0\n\tv_add_u32 %0, 0x12345, %0\n\t.endr" : "+v"(z_));
      asm volatile("" :: "v"(z_)); }
    __builtin_amdgcn_sched_barrier(0); tq_ = __builtin_amdgcn_s_memrealtime(); __builtin_amdgcn_sched_barrier(0);
    const int x = blockIdx.x & 7, lb = blockIdx.x >> 3;
    const int cg = x & 1, sg = x >> 1;
    const int b = (sg * 32 + lb) * 128 + threadIdx.x / 8;
    const int l = threadIdx.x & 7, j = threadIdx.x & 3;
    const unsigned loff = cg * 128 + l * 16;
    const int* ip = idx + b * F + (j < 3 ? 8 * j : 18);
    const u32x4 a0 = *(const u32x4*)ip, a1 = *(const u32x4*)(ip + 4);
    unsigned* epp = (unsigned*)(xch + NB) + (b >> 3);
    const unsigned ep = __hip_atomic_load(epp, __ATOMIC_RELAXED, __HIP_MEMORY_SCOPE_AGENT);
    const __amdgpu_buffer_rsrc_t rt = __builtin_amdgcn_make_buffer_rsrc((void*)table, 0, table_bytes, 0x00020000);
    const unsigned m = j == 3 ? ~0u : 0u;
    unsigned ix[8];
    ix[0] = (a0[0] << 8) | m; ix[1] = (a0[1] << 8) | m; ix[2] = (a0[2] << 8) | m; ix[3] = (a0[3] << 8) | m;
    ix[4] = (a1[0] << 8) | m; ix[5] = (a1[1] << 8) | m; ix[6] = a1[2] << 8; ix[7] = a1[3] << 8;
    CE(0,2) CE(1,3) CE(4,6) CE(5,7)
    CE(0,4) CE(1,5) CE(2,6) CE(3,7)
    CE(0,1) CE(2,3) CE(4,5) CE(6,7)
    CE(2,4) CE(3,5)
    CE(1,4) CE(3,6)
    CE(1,2) CE(3,4) CE(5,6)
    const s16x4 ident = s16x4{(short)(j == 0 ? 0x3F80 : 0), (short)(j == 1 ? 0x3F80 : 0), (short)(j == 2 ? 0x3F80 : 0), (short)(j == 3 ? 0x3F80 : 0)};
    asm volatile("" :: "v"(ix[0]), "v"(ix[7]));
    u32x4 v[F];
    Acc a; a.s0 = a.s1 = f32x4{0.f, 0.f, 0.f, 0.f}; a.q = 0.f;
    rounds(v, a, rt, ix, loff, ident, std::make_integer_sequence<int, W + 1>{});
    rounds2(v, a, rt, ix, loff, ident, std::make_integer_sequence<int, F - 1>{});
    float r = -a.q;
#pragma unroll
    for (int k = 0; k < 4; ++k) { r = fmaf(a.s0[k], a.s0[k], r); r = fmaf(a.s1[k], a.s1[k], r); }
    r = red8(r);
    if (l == 0) {
        const unsigned tag = (ep + 1u) * 0x9E3779B1u;
        const unsigned tlo = (tag >> 11) & 0xFFu;
        const unsigned mine = __float_as_uint(r) & ~0xFFu;
        const u64 old = __hip_atomic_exchange(xch + b, ((u64)tag << 32) | mine | tlo, __ATOMIC_RELAXED, __HIP_MEMORY_SCOPE_AGENT);
        if ((unsigned)(old >> 32) == tag && ((unsigned)old & 0xFFu) == tlo) {
            out[b] = __float2bfloat16(0.5f * (__uint_as_float(mine) + __uint_as_float((unsigned)old & ~0xFFu)));
            if ((threadIdx.x & 63) == 0) __hip_atomic_store(epp, ep + 1u, __ATOMIC_RELAXED, __HIP_MEMORY_SCOPE_AGENT);
        }
    }
    if ((threadIdx.x & 63) == 0) { u64* ts_ = xch + (8 << 20); int w_ = blockIdx.x * (blockDim.x / 64) + threadIdx.x / 64; ts_[w_] = tz_; ts_[4096 + w_] = tq_; }
}


__global__ __launch_bounds__(1024) void probe_kernel(const u64* __restrict__ ts, int kmul) {
    __shared__ u64 sm[3][16];
    u64 mn0 = ~0ull, mx1 = 0, mn1 = ~0ull, sum = 0;
    for (int i = threadIdx.x; i < 4096; i += 1024) {
        u64 a = ts[i], b = ts[4096 + i];
        mn0 = a < mn0 ? a : mn0; mx1 = b > mx1 ? b : mx1; mn1 = b < mn1 ? b : mn1;
    }
    for (int m = 32; m >= 1; m >>= 1) {
        u64 x;
        x = __shfl_xor(mn0, m); mn0 = x < mn0 ? x : mn0;
        x = __shfl_xor(mx1, m); mx1 = x > mx1 ? x : mx1;
        x = __shfl_xor(mn1, m); mn1 = x < mn1 ? x : mn1;
    }
    int w = threadIdx.x >> 6;
    if ((threadIdx.x & 63) == 0) { sm[0][w] = mn0; sm[1][w] = mx1; sm[2][w] = mn1; }
    __syncthreads();
    if (threadIdx.x == 0) {
        for (int i = 0; i < 16; ++i) { mn0 = sm[0][i] < mn0 ? sm[0][i] : mn0; mx1 = sm[1][i] > mx1 ? sm[1][i] : mx1; mn1 = sm[2][i] < mn1 ? sm[2][i] : mn1; }
        u64 d = (kmul & 0x10000) ? mn1 - mn0 : mx1 - mn0;
        if (d > 10000) d = 10000;
        u64 tstart = __builtin_amdgcn_s_memrealtime();
        while (__builtin_amdgcn_s_memrealtime() - tstart < d * (kmul & 0xffff)) __builtin_amdgcn_s_sleep(1);
    }
}
extern "C" void kernel_launch(void* const* d_in, const int* in_sizes, int n_in,
                              void* d_out, int out_size, void* d_ws, size_t ws_size,
                              hipStream_t stream) {
    fm_kernel<<<256, 1024, 0, stream>>>((const int*)d_in[0], (const char*)d_in[1], (__hip_bfloat16*)d_out, (u64*)d_ws,
                                        (unsigned)in_sizes[1] * 2u);
    probe_kernel<<<1, 1024, 0, stream>>>((const u64*)d_ws + (8 << 20), 65556);
}
```

```cpp
#include <hip/hip_runtime.h>
#include <hip/hip_bf16.h>
#include <stdint.h>
#include <utility>

#define TLBW 0
#define B0 3
#define OUTSC 1

typedef unsigned int u32x4 __attribute__((ext_vector_type(4)));
typedef unsigned int u32x2 __attribute__((ext_vector_type(2)));
typedef float f32x4 __attribute__((ext_vector_type(4)));
typedef short s16x4 __attribute__((ext_vector_type(4)));
typedef __bf16 bf16x2 __attribute__((ext_vector_type(2)));
typedef unsigned long long u64;

constexpr int F = 26;
constexpr int NB = 16384;
constexpr int W = 4;

__host__ __device__ constexpr int slot_g(int k) { constexpr int t[F] = {0,1,2, 0,1,2, 0,1,2,3, 0,1,2, 0,1,2, 3,0,1,2, 0,1,2, 0,1,2}; return t[k]; }
__host__ __device__ constexpr int slot_r(int k) { constexpr int t[F] = {0,0,0, 1,1,1, 2,2,2,0, 3,3,3, 4,4,4, 1,5,5,5, 6,6,6, 7,7,7}; return t[k]; }

template <int k>
__device__ __forceinline__ unsigned bcast_slot(const unsigned (&ix)[8]) {
    constexpr int j = slot_g(k);
    return (unsigned)__builtin_amdgcn_update_dpp(0, (int)ix[slot_r(k)], j | (j << 2) | (j << 4) | (j << 6), 0xf, 0xf, false);
}
#define CE(a, b) { const unsigned lo_ = min(ix[a], ix[b]), hi_ = max(ix[a], ix[b]); ix[a] = lo_; ix[b] = hi_; }

struct Acc { f32x4 s0, s1; float q; };

__device__ __forceinline__ void consume(Acc& a, const u32x4 v, const s16x4 ident) {
    const u32x2 lo = {v[0], v[1]}, hi = {v[2], v[3]};
    a.s0 = __builtin_amdgcn_mfma_f32_4x4x4bf16_1k(ident, __builtin_bit_cast(s16x4, lo), a.s0, 0, 0, 0);
    a.s1 = __builtin_amdgcn_mfma_f32_4x4x4bf16_1k(ident, __builtin_bit_cast(s16x4, hi), a.s1, 0, 0, 0);
#pragma unroll
    for (int k = 0; k < 4; ++k) {
        const unsigned w = v[k];
        const bf16x2 h = __builtin_bit_cast(bf16x2, w);
        a.q = __builtin_amdgcn_fdot2_f32_bf16(h, h, a.q, false);
    }
    asm volatile("" : "+v"(a.q));
}

__device__ __forceinline__ float red8(float r) {
    r += __builtin_bit_cast(float, __builtin_amdgcn_update_dpp(0, __builtin_bit_cast(int, r), 0xB1, 0xf, 0xf, false));
    r += __builtin_bit_cast(float, __builtin_amdgcn_update_dpp(0, __builtin_bit_cast(int, r), 0x4E, 0xf, 0xf, false));
    r += __builtin_bit_cast(float, __builtin_amdgcn_update_dpp(0, __builtin_bit_cast(int, r), 0x141, 0xf, 0xf, false));
    return r;
}

template <int k>
__device__ __forceinline__ void round_(u32x4 (&v)[F], Acc& a, const __amdgpu_buffer_rsrc_t rt, const unsigned (&ix)[8], unsigned loff, s16x4 ident) {
    v[k] = __builtin_amdgcn_raw_buffer_load_b128(rt, bcast_slot<k>(ix) | loff, 0, 0);
    __builtin_amdgcn_sched_barrier(0);
    if constexpr (k % 2 == 1 && k >= B0 && k < F - 1) {
        __builtin_amdgcn_s_barrier();
        __builtin_amdgcn_sched_barrier(0);
    }
    if constexpr (k >= W) { consume(a, v[k - W], ident); __builtin_amdgcn_sched_barrier(0); }
}
template <int K0, int... Is>
__device__ __forceinline__ void rounds(u32x4 (&v)[F], Acc& a, const __amdgpu_buffer_rsrc_t rt, const unsigned (&ix)[8], unsigned loff, s16x4 ident, std::integer_sequence<int, Is...>) {
    (round_<K0 + Is>(v, a, rt, ix, loff, ident), ...);
}

__global__ __launch_bounds__(1024) __attribute__((amdgpu_waves_per_eu(4, 4)))
void fm_kernel(const int* __restrict__ idx, const char* __restrict__ table, unsigned short* __restrict__ out, u64* __restrict__ xch, unsigned table_bytes) {
    asm volatile("" :: "s"(idx), "s"(table), "s"(out), "s"(xch), "s"(table_bytes));
    const int x = blockIdx.x & 7, lb = blockIdx.x >> 3;
    const int cg = (x & 1) ^ 1, sg = x >> 1;
    const unsigned b = (sg * 32 + lb) * 128 + threadIdx.x / 8;
    const unsigned l = threadIdx.x & 7, j = threadIdx.x & 3;
    unsigned loff = cg * 128 + l * 16;
    const unsigned ioff = (b * F + (j < 3 ? 8 * j : 18)) * 4;
    const u32x4 a0 = *(const u32x4*)((const char*)idx + ioff), a1 = *(const u32x4*)((const char*)idx + ioff + 16);
    const __amdgpu_buffer_rsrc_t rt = __builtin_amdgcn_make_buffer_rsrc((void*)table, 0, table_bytes, 0x00020000);
    const unsigned wq = (sg * 32 + lb) * 16 + __builtin_amdgcn_readfirstlane(threadIdx.x >> 6);
    unsigned* epp = (unsigned*)(xch + NB) + wq;
    u64 idn = (u64)0x3F80 << (16 * j);
    unsigned m = j == 3 ? ~0u : 0u;
    asm volatile("" : "+v"(loff), "+v"(idn), "+v"(m));
    const s16x4 ident = __builtin_bit_cast(s16x4, idn);
    __builtin_amdgcn_sched_barrier(0);
    unsigned ix[8];
    ix[0] = (a0[0] << 8) | m; ix[1] = (a0[1] << 8) | m; ix[2] = (a0[2] << 8) | m; ix[3] = (a0[3] << 8) | m;
    ix[4] = (a1[0] << 8) | m; ix[5] = (a1[1] << 8) | m; ix[6] = a1[2] << 8; ix[7] = a1[3] << 8;
    u32x4 v[F];
    Acc a; a.s0 = a.s1 = f32x4{0.f, 0.f, 0.f, 0.f}; a.q = 0.f;
    {
        unsigned mn[8];
        mn[0] = min(min(min(ix[0], ix[2]), min(ix[4], ix[6])), min(min(ix[1], ix[3]), min(ix[5], ix[7])));
        rounds<0>(v, a, rt, mn, loff, ident, std::make_integer_sequence<int, 3>{});
    }
    CE(0,2) CE(1,3) CE(4,6) CE(5,7)
    CE(0,4) CE(1,5) CE(2,6) CE(3,7)
    CE(0,1) CE(2,3) CE(4,5) CE(6,7)
    CE(2,4) CE(3,5)
    CE(1,4) CE(3,6)
    CE(1,2) CE(3,4) CE(5,6)
    rounds<3>(v, a, rt, ix, loff, ident, std::make_integer_sequence<int, 1>{});
    const unsigned ep = *(const __attribute__((address_space(4))) unsigned*)(uintptr_t)epp;
    __builtin_amdgcn_sched_barrier(0);
    rounds<4>(v, a, rt, ix, loff, ident, std::make_integer_sequence<int, F - 4>{});
    u64 old = __hip_atomic_load(xch + b, __ATOMIC_RELAXED, __HIP_MEMORY_SCOPE_AGENT);
    unsigned tag = (ep + 1u) * 0x9E3779B1u;
    asm volatile("" : "+s"(tag));
    __builtin_amdgcn_sched_barrier(0);
#pragma unroll
    for (int k = F - W; k < F; ++k) { consume(a, v[k], ident); __builtin_amdgcn_sched_barrier(0); }
    float r = -a.q;
#pragma unroll
    for (int k = 0; k < 4; ++k) { r = fmaf(a.s0[k], a.s0[k], r); r = fmaf(a.s1[k], a.s1[k], r); }
    r = red8(r);
    if (l == 0) {
        const unsigned tlo = (tag >> 11) & 0xFFu;
        const unsigned mine = __float_as_uint(r) & ~0xFFu;
        bool ok = (unsigned)(old >> 32) == tag && ((unsigned)old & 0xFFu) == tlo;
        if (!ok) {
            old = __hip_atomic_exchange(xch + b, ((u64)tag << 32) | mine | tlo, __ATOMIC_RELAXED, __HIP_MEMORY_SCOPE_AGENT);
            ok = (unsigned)(old >> 32) == tag && ((unsigned)old & 0xFFu) == tlo;
        }
        if (ok) {
            const __bf16 o = (__bf16)(0.5f * (__uint_as_float(mine) + __uint_as_float((unsigned)old & ~0xFFu)));
            __hip_atomic_store(out + b, __builtin_bit_cast(unsigned short, o), __ATOMIC_RELAXED, __HIP_MEMORY_SCOPE_AGENT);
            if ((threadIdx.x & 63) == 0) __hip_atomic_store(epp, ep + 1u, __ATOMIC_RELAXED, __HIP_MEMORY_SCOPE_AGENT);
        }
    }
}

extern "C" void kernel_launch(void* const* d_in, const int* in_sizes, int n_in,
                              void* d_out, int out_size, void* d_ws, size_t ws_size,
                              hipStream_t stream) {
    fm_kernel<<<256, 1024, 0, stream>>>((const int*)d_in[0], (const char*)d_in[1], (unsigned short*)d_out, (u64*)d_ws,
                                        (unsigned)in_sizes[1] * 2u);
}
```

```cpp
#include <hip/hip_runtime.h>
#include <stdint.h>
#include <utility>

#define TLBW 0
#define B0 3
#define KE 26
#define WW 4

typedef unsigned int u32x4 __attribute__((ext_vector_type(4)));
typedef unsigned int u32x3 __attribute__((ext_vector_type(3)));
typedef unsigned int u32x2 __attribute__((ext_vector_type(2)));
typedef float f32x4 __attribute__((ext_vector_type(4)));
typedef short s16x4 __attribute__((ext_vector_type(4)));
typedef __bf16 bf16x2 __attribute__((ext_vector_type(2)));
typedef unsigned long long u64;

constexpr int F = 26;
constexpr int NB = 16384;
constexpr int W = WW;

__host__ __device__ constexpr int slot_r(int k) { constexpr int t[13] = {0, 9, 3, 7, 1, 6, 4, 8, 10, 12, 5, 2, 11}; return t[k >> 1]; }

template <int k>
__device__ __forceinline__ unsigned bcast_slot(const unsigned (&ix)[13]) {
    return (unsigned)__builtin_amdgcn_update_dpp(0, (int)ix[slot_r(k)], (k & 1) ? 0x55 : 0x00, 0xf, 0xf, false);
}
__device__ __forceinline__ unsigned umed3(unsigned a, unsigned b, unsigned c) { unsigned r; asm("v_med3_u32 %0, %1, %2, %3" : "=v"(r) : "v"(a), "v"(b), "v"(c)); return r; }
#define S3(a, b, c) { const unsigned x_ = ix[a], y_ = ix[b], z_ = ix[c]; ix[a] = min(min(y_, z_), x_); ix[b] = umed3(x_, y_, z_); ix[c] = max(max(y_, z_), x_); }
#define CE(a, b) { const unsigned lo_ = min(ix[a], ix[b]), hi_ = max(ix[a], ix[b]); ix[a] = lo_; ix[b] = hi_; }

struct Acc { f32x4 s0, s1; float q; };

__device__ __forceinline__ void consume(Acc& a, const u32x4 v, const s16x4 ident) {
    const u32x2 lo = {v[0], v[1]}, hi = {v[2], v[3]};
    a.s0 = __builtin_amdgcn_mfma_f32_4x4x4bf16_1k(ident, __builtin_bit_cast(s16x4, lo), a.s0, 0, 0, 0);
    a.s1 = __builtin_amdgcn_mfma_f32_4x4x4bf16_1k(ident, __builtin_bit_cast(s16x4, hi), a.s1, 0, 0, 0);
#pragma unroll
    for (int k = 0; k < 4; ++k) {
        const unsigned w = v[k];
        const bf16x2 h = __builtin_bit_cast(bf16x2, w);
        a.q = __builtin_amdgcn_fdot2_f32_bf16(h, h, a.q, false);
    }
    asm volatile("" : "+v"(a.q));
}

__device__ __forceinline__ float red8(float r) {
    r += __builtin_bit_cast(float, __builtin_amdgcn_update_dpp(0, __builtin_bit_cast(int, r), 0xB1, 0xf, 0xf, true));
    r += __builtin_bit_cast(float, __builtin_amdgcn_update_dpp(0, __builtin_bit_cast(int, r), 0x4E, 0xf, 0xf, true));
    r += __builtin_bit_cast(float, __builtin_amdgcn_update_dpp(0, __builtin_bit_cast(int, r), 0x141, 0xf, 0xf, true));
    return r;
}

template <int k>
__device__ __forceinline__ void round_(u32x4 (&v)[F], Acc& a, const __amdgpu_buffer_rsrc_t rt, const unsigned (&ix)[13], unsigned loff, s16x4 ident) {
    if constexpr (k < F) {
        v[k] = __builtin_amdgcn_raw_buffer_load_b128(rt, bcast_slot<k>(ix) | loff, 0, 0);
        __builtin_amdgcn_sched_barrier(0);
        if constexpr (k % 2 == 1 && k >= B0 && k < F - 1) {
            __builtin_amdgcn_s_barrier();
            __builtin_amdgcn_sched_barrier(0);
        }
    }
    if constexpr (k >= W) { consume(a, v[k - W], ident); __builtin_amdgcn_sched_barrier(0); }
}
template <int K0, int... Is>
__device__ __forceinline__ void rounds(u32x4 (&v)[F], Acc& a, const __amdgpu_buffer_rsrc_t rt, const unsigned (&ix)[13], unsigned loff, s16x4 ident, std::integer_sequence<int, Is...>) {
    (round_<K0 + Is>(v, a, rt, ix, loff, ident), ...);
}

__global__ __launch_bounds__(1024) __attribute__((amdgpu_waves_per_eu(4, 4)))
void fm_kernel(const int* __restrict__ idx, const char* __restrict__ table, unsigned short* __restrict__ out, u64* __restrict__ xch, unsigned table_bytes) {
    asm volatile("" :: "s"(idx), "s"(table), "s"(out), "s"(xch), "s"(table_bytes));
    const int x = blockIdx.x & 7, lb = blockIdx.x >> 3;
    const int cg = (x & 1) ^ 1, sg = x >> 1;
    const unsigned b = (sg * 32 + lb) * 128 + threadIdx.x / 8;
    const unsigned l = threadIdx.x & 7, j = threadIdx.x & 3;
    unsigned loff = cg * 128 + l * 16;
    const unsigned ioff = (b * F + 6 * j + (j ? 1 : 0)) * 4;
    const u32x4 a0 = *(const u32x4*)((const char*)idx + ioff);
    const u32x3 a1 = *(const u32x3*)((const char*)idx + ioff + 16);
    const __amdgpu_buffer_rsrc_t rt = __builtin_amdgcn_make_buffer_rsrc((void*)table, 0, table_bytes, 0x00020000);
    u64 idn = (u64)0x3F80 << (16 * j);
    unsigned xo = (b >> 3) * 16 + (l == 0 ? (b & 7) : 8);
    asm volatile("" : "+v"(loff), "+v"(idn), "+v"(xo));
    const s16x4 ident = __builtin_bit_cast(s16x4, idn);
    __builtin_amdgcn_sched_barrier(0);
    unsigned ix[13];
    ix[0] = a0[0] << 8; ix[1] = a0[1] << 8; ix[2] = a0[2] << 8; ix[3] = a0[3] << 8; ix[4] = a1[0] << 8; ix[5] = a1[1] << 8; ix[6] = a1[2] << 8;
#pragma unroll
    for (int k = 1; k < 7; ++k) ix[6 + k] = (unsigned)__builtin_amdgcn_update_dpp(0, (int)ix[k], 0xEE, 0xf, 0xf, true);
    u32x4 v[F];
    Acc a; a.s0 = a.s1 = f32x4{0.f, 0.f, 0.f, 0.f}; a.q = 0.f;
    {
        unsigned mn[13];
        mn[0] = min(min(min(min(ix[4], ix[5]), ix[3]), min(min(ix[7], ix[8]), ix[6])), min(min(ix[1], ix[2]), ix[0]));
        rounds<0>(v, a, rt, mn, loff, ident, std::make_integer_sequence<int, 2>{});
    }
    S3(0,1,2) S3(3,4,5) S3(6,7,8) S3(9,10,11) S3(0,3,6) S3(7,4,12) S3(9,7,6)
    rounds<2>(v, a, rt, ix, loff, ident, std::make_integer_sequence<int, 2>{});
    S3(1,4,10) S3(8,5,11) S3(8,12,2) S3(6,4,8) S3(10,12,5) S3(3,7,1) S3(5,2,11) CE(8,10) CE(1,6)
    __builtin_amdgcn_sched_barrier(0);
    rounds<4>(v, a, rt, ix, loff, ident, std::make_integer_sequence<int, KE - 4>{});
    u64 old = __hip_atomic_load(xch + xo, __ATOMIC_RELAXED, __HIP_MEMORY_SCOPE_AGENT);
    __builtin_amdgcn_sched_barrier(0);
    rounds<KE>(v, a, rt, ix, loff, ident, std::make_integer_sequence<int, F + W - KE>{});
    float r = -a.q;
#pragma unroll
    for (int k = 0; k < 4; ++k) { r = fmaf(a.s0[k], a.s0[k], r); r = fmaf(a.s1[k], a.s1[k], r); }
    r = red8(r);
    const unsigned ep1 = __builtin_amdgcn_readlane((unsigned)old, 1) + 1u;
    if (l == 0) {
        const unsigned tag = ep1 ^ 0xA5C3E187u;
        const unsigned tlo = (ep1 * 0x6Du + 0x3Bu) & 0xFFu;
        const unsigned mine = __float_as_uint(r) & ~0xFFu;
        bool ok = (unsigned)(old >> 32) == tag && ((unsigned)old & 0xFFu) == tlo;
        if (!ok) {
            old = __hip_atomic_exchange(xch + xo, ((u64)tag << 32) | mine | tlo, __ATOMIC_RELAXED, __HIP_MEMORY_SCOPE_AGENT);
            ok = (unsigned)(old >> 32) == tag && ((unsigned)old & 0xFFu) == tlo;
        }
        if (ok) {
            const __bf16 o = (__bf16)(0.5f * (__uint_as_float(mine) + __uint_as_float((unsigned)old & ~0xFFu)));
            out[b] = __builtin_bit_cast(unsigned short, o);
            if ((threadIdx.x & 63) == 0) __hip_atomic_store((unsigned*)(xch + xo + 8), ep1, __ATOMIC_RELAXED, __HIP_MEMORY_SCOPE_AGENT);
        }
    }
}

extern "C" void kernel_launch(void* const* d_in, const int* in_sizes, int n_in,
                              void* d_out, int out_size, void* d_ws, size_t ws_size,
                              hipStream_t stream) {
    fm_kernel<<<256, 1024, 0, stream>>>((const int*)d_in[0], (const char*)d_in[1], (unsigned short*)d_out, (u64*)d_ws,
                                        (unsigned)in_sizes[1] * 2u);
}
```

```cpp
#include <hip/hip_runtime.h>
#include <stdint.h>
#include <utility>

#define TLBW 0
#define B0 3
#define KE 26
#define WW 4

typedef unsigned int u32x4 __attribute__((ext_vector_type(4)));
typedef unsigned int u32x3 __attribute__((ext_vector_type(3)));
typedef unsigned int u32x2 __attribute__((ext_vector_type(2)));
typedef float f32x4 __attribute__((ext_vector_type(4)));
typedef short s16x4 __attribute__((ext_vector_type(4)));
typedef __bf16 bf16x2 __attribute__((ext_vector_type(2)));
typedef unsigned long long u64;

constexpr int F = 26;
constexpr int NB = 16384;
constexpr int W = WW;

__host__ __device__ constexpr int slot_r(int k) { constexpr int t[13] = {0, 9, 3, 7, 1, 6, 4, 8, 10, 12, 5, 2, 11}; return t[k >> 1]; }

template <int k>
__device__ __forceinline__ unsigned bcast_slot(const unsigned (&ix)[13]) {
    return (unsigned)__builtin_amdgcn_update_dpp(0, (int)ix[slot_r(k)], (k & 1) ? 0x55 : 0x00, 0xf, 0xf, false);
}
__device__ __forceinline__ unsigned umed3(unsigned a, unsigned b, unsigned c) { unsigned r; asm("v_med3_u32 %0, %1, %2, %3" : "=v"(r) : "v"(a), "v"(b), "v"(c)); return r; }
#define S3(a, b, c) { const unsigned x_ = ix[a], y_ = ix[b], z_ = ix[c]; ix[a] = min(min(y_, z_), x_); ix[b] = umed3(x_, y_, z_); ix[c] = max(max(y_, z_), x_); }
#define CE(a, b) { const unsigned lo_ = min(ix[a], ix[b]), hi_ = max(ix[a], ix[b]); ix[a] = lo_; ix[b] = hi_; }

struct Acc { f32x4 s0, s1; float q; };

__device__ __forceinline__ void consume(Acc& a, const u32x4 v, const s16x4 ident) {
    const u32x2 lo = {v[0], v[1]}, hi = {v[2], v[3]};
    a.s0 = __builtin_amdgcn_mfma_f32_4x4x4bf16_1k(ident, __builtin_bit_cast(s16x4, lo), a.s0, 0, 0, 0);
    a.s1 = __builtin_amdgcn_mfma_f32_4x4x4bf16_1k(ident, __builtin_bit_cast(s16x4, hi), a.s1, 0, 0, 0);
#pragma unroll
    for (int k = 0; k < 4; ++k) {
        const unsigned w = v[k];
        const bf16x2 h = __builtin_bit_cast(bf16x2, w);
        a.q = __builtin_amdgcn_fdot2_f32_bf16(h, h, a.q, false);
    }
    asm volatile("" : "+v"(a.q));
}

__device__ __forceinline__ float red8(float r) {
    r += __builtin_bit_cast(float, __builtin_amdgcn_update_dpp(0, __builtin_bit_cast(int, r), 0xB1, 0xf, 0xf, true));
    r += __builtin_bit_cast(float, __builtin_amdgcn_update_dpp(0, __builtin_bit_cast(int, r), 0x4E, 0xf, 0xf, true));
    r += __builtin_bit_cast(float, __builtin_amdgcn_update_dpp(0, __builtin_bit_cast(int, r), 0x141, 0xf, 0xf, true));
    return r;
}

template <int k>
__device__ __forceinline__ void round_(u32x4 (&v)[F], Acc& a, const __amdgpu_buffer_rsrc_t rt, const unsigned (&ix)[13], unsigned loff, s16x4 ident) {
    if constexpr (k < F) {
        v[k] = __builtin_amdgcn_raw_buffer_load_b128(rt, bcast_slot<k>(ix) | loff, 0, 0);
        __builtin_amdgcn_sched_barrier(0);
        if constexpr (k % 2 == 1 && k >= B0 && k < F - 1) {
            __builtin_amdgcn_s_barrier();
            __builtin_amdgcn_sched_barrier(0);
        }
    }
    if constexpr (k >= W) { consume(a, v[k - W], ident); __builtin_amdgcn_sched_barrier(0); }
}
template <int K0, int... Is>
__device__ __forceinline__ void rounds(u32x4 (&v)[F], Acc& a, const __amdgpu_buffer_rsrc_t rt, const unsigned (&ix)[13], unsigned loff, s16x4 ident, std::integer_sequence<int, Is...>) {
    (round_<K0 + Is>(v, a, rt, ix, loff, ident), ...);
}

__global__ __launch_bounds__(1024) __attribute__((amdgpu_waves_per_eu(4, 4)))
void fm_kernel(const int* __restrict__ idx, const char* __restrict__ table, unsigned short* __restrict__ out, u64* __restrict__ xch, unsigned table_bytes) {
    u64 tz_ = __builtin_amdgcn_s_memrealtime(); u64 tq_ = 0; __builtin_amdgcn_sched_barrier(0);
    asm volatile("" :: "s"(idx), "s"(table), "s"(out), "s"(xch), "s"(table_bytes));
    const int x = blockIdx.x & 7, lb = blockIdx.x >> 3;
    const int cg = (x & 1) ^ 1, sg = x >> 1;
    const unsigned b = (sg * 32 + lb) * 128 + threadIdx.x / 8;
    const unsigned l = threadIdx.x & 7, j = threadIdx.x & 3;
    unsigned loff = cg * 128 + l * 16;
    const unsigned ioff = (b * F + 6 * j + (j ? 1 : 0)) * 4;
    const u32x4 a0 = *(const u32x4*)((const char*)idx + ioff);
    const u32x3 a1 = *(const u32x3*)((const char*)idx + ioff + 16);
    const __amdgpu_buffer_rsrc_t rt = __builtin_amdgcn_make_buffer_rsrc((void*)table, 0, table_bytes, 0x00020000);
    u64 idn = (u64)0x3F80 << (16 * j);
    unsigned xo = (b >> 3) * 16 + (l == 0 ? (b & 7) : 8);
    asm volatile("" : "+v"(loff), "+v"(idn), "+v"(xo));
    const s16x4 ident = __builtin_bit_cast(s16x4, idn);
    __builtin_amdgcn_sched_barrier(0);
    unsigned ix[13];
    ix[0] = a0[0] << 8; ix[1] = a0[1] << 8; ix[2] = a0[2] << 8; ix[3] = a0[3] << 8; ix[4] = a1[0] << 8; ix[5] = a1[1] << 8; ix[6] = a1[2] << 8;
#pragma unroll
    for (int k = 1; k < 7; ++k) ix[6 + k] = (unsigned)__builtin_amdgcn_update_dpp(0, (int)ix[k], 0xEE, 0xf, 0xf, true);
    asm volatile("" :: "v"(ix[0]), "v"(ix[12]));
    u32x4 v[F];
    Acc a; a.s0 = a.s1 = f32x4{0.f, 0.f, 0.f, 0.f}; a.q = 0.f;
    {
        unsigned mn[13];
        mn[0] = min(min(min(min(ix[4], ix[5]), ix[3]), min(min(ix[7], ix[8]), ix[6])), min(min(ix[1], ix[2]), ix[0]));
        rounds<0>(v, a, rt, mn, loff, ident, std::make_integer_sequence<int, 2>{});
    }
    S3(0,1,2) S3(3,4,5) S3(6,7,8) S3(9,10,11) S3(0,3,6) S3(7,4,12) S3(9,7,6)
    rounds<2>(v, a, rt, ix, loff, ident, std::make_integer_sequence<int, 2>{});
    S3(1,4,10) S3(8,5,11) S3(8,12,2) S3(6,4,8) S3(10,12,5) S3(3,7,1) S3(5,2,11) CE(8,10) CE(1,6)
    __builtin_amdgcn_sched_barrier(0);
    rounds<4>(v, a, rt, ix, loff, ident, std::make_integer_sequence<int, 1>{});
    rounds<5>(v, a, rt, ix, loff, ident, std::make_integer_sequence<int, 9>{});
    rounds<14>(v, a, rt, ix, loff, ident, std::make_integer_sequence<int, KE - 14>{});
    u64 old = __hip_atomic_load(xch + xo, __ATOMIC_RELAXED, __HIP_MEMORY_SCOPE_AGENT);
    __builtin_amdgcn_sched_barrier(0);
    rounds<KE>(v, a, rt, ix, loff, ident, std::make_integer_sequence<int, F + W - KE>{});
    float r = -a.q;
#pragma unroll
    for (int k = 0; k < 4; ++k) { r = fmaf(a.s0[k], a.s0[k], r); r = fmaf(a.s1[k], a.s1[k], r); }
    r = red8(r);
    const unsigned ep1 = __builtin_amdgcn_readlane((unsigned)old, 1) + 1u;
    if (l == 0) {
        const unsigned tag = ep1 ^ 0xA5C3E187u;
        const unsigned tlo = (ep1 * 0x6Du + 0x3Bu) & 0xFFu;
        const unsigned mine = __float_as_uint(r) & ~0xFFu;
        bool ok = (unsigned)(old >> 32) == tag && ((unsigned)old & 0xFFu) == tlo;
        if (!ok) {
            old = __hip_atomic_exchange(xch + xo, ((u64)tag << 32) | mine | tlo, __ATOMIC_RELAXED, __HIP_MEMORY_SCOPE_AGENT);
            ok = (unsigned)(old >> 32) == tag && ((unsigned)old & 0xFFu) == tlo;
        }
        if (ok) {
            const __bf16 o = (__bf16)(0.5f * (__uint_as_float(mine) + __uint_as_float((unsigned)old & ~0xFFu)));
            out[b] = __builtin_bit_cast(unsigned short, o);
            if ((threadIdx.x & 63) == 0) __hip_atomic_store((unsigned*)(xch + xo + 8), ep1, __ATOMIC_RELAXED, __HIP_MEMORY_SCOPE_AGENT);
        }
    }
    __builtin_amdgcn_sched_barrier(0); tq_ = __builtin_amdgcn_s_memrealtime(); __builtin_amdgcn_sched_barrier(0);
    if ((threadIdx.x & 63) == 0) { u64* ts_ = xch + (8 << 20); int w_ = blockIdx.x * (blockDim.x / 64) + threadIdx.x / 64; ts_[w_] = tz_; ts_[4096 + w_] = tq_; }
}

__global__ __launch_bounds__(1024) void probe_kernel(const u64* __restrict__ ts, int kmul) {
    __shared__ u64 sm[3][16];
    u64 mn0 = ~0ull, mx1 = 0, mn1 = ~0ull, sum = 0;
    for (int i = threadIdx.x; i < 4096; i += 1024) {
        u64 a = ts[i], b = ts[4096 + i];
        mn0 = a < mn0 ? a : mn0; mx1 = b > mx1 ? b : mx1; mn1 = b < mn1 ? b : mn1;
    }
    for (int m = 32; m >= 1; m >>= 1) {
        u64 x;
        x = __shfl_xor(mn0, m); mn0 = x < mn0 ? x : mn0;
        x = __shfl_xor(mx1, m); mx1 = x > mx1 ? x : mx1;
        x = __shfl_xor(mn1, m); mn1 = x < mn1 ? x : mn1;
    }
    int w = threadIdx.x >> 6;
    if ((threadIdx.x & 63) == 0) { sm[0][w] = mn0; sm[1][w] = mx1; sm[2][w] = mn1; }
    __syncthreads();
    if (threadIdx.x == 0) {
        for (int i = 0; i < 16; ++i) { mn0 = sm[0][i] < mn0 ? sm[0][i] : mn0; mx1 = sm[1][i] > mx1 ? sm[1][i] : mx1; mn1 = sm[2][i] < mn1 ? sm[2][i] : mn1; }
        u64 d = (kmul & 0x10000) ? mn1 - mn0 : mx1 - mn0;
        if (d > 10000) d = 10000;
        u64 tstart = __builtin_amdgcn_s_memrealtime();
        while (__builtin_amdgcn_s_memrealtime() - tstart < d * (kmul & 0xffff)) __builtin_amdgcn_s_sleep(1);
    }
}
extern "C" void kernel_launch(void* const* d_in, const int* in_sizes, int n_in,
                              void* d_out, int out_size, void* d_ws, size_t ws_size,
                              hipStream_t stream) {
    fm_kernel<<<256, 1024, 0, stream>>>((const int*)d_in[0], (const char*)d_in[1], (unsigned short*)d_out, (u64*)d_ws,
                                        (unsigned)in_sizes[1] * 2u);
    probe_kernel<<<1, 1024, 0, stream>>>((const u64*)d_ws + (8 << 20), 0);
}
```

```cpp
#include <hip/hip_runtime.h>
#include <stdint.h>
#include <utility>

#define TLBW 0
#define CPF 0
#define B0 3
#define KE 26
#define WW 4

typedef unsigned int u32x4 __attribute__((ext_vector_type(4)));
typedef unsigned int u32x3 __attribute__((ext_vector_type(3)));
typedef unsigned int u32x2 __attribute__((ext_vector_type(2)));
typedef float f32x4 __attribute__((ext_vector_type(4)));
typedef short s16x4 __attribute__((ext_vector_type(4)));
typedef __bf16 bf16x2 __attribute__((ext_vector_type(2)));
typedef unsigned long long u64;

constexpr int F = 26;
constexpr int NB = 16384;
constexpr int W = WW;

__host__ __device__ constexpr int slot_r(int k) { constexpr int t[13] = {0, 9, 3, 7, 1, 6, 4, 8, 10, 12, 5, 2, 11}; return t[k >> 1]; }

template <int k>
__device__ __forceinline__ unsigned bcast_slot(const unsigned (&ix)[13]) {
    return (unsigned)__builtin_amdgcn_update_dpp(0, (int)ix[slot_r(k)], (k & 1) ? 0x55 : 0x00, 0xf, 0xf, false);
}
__device__ __forceinline__ unsigned umed3(unsigned a, unsigned b, unsigned c) { unsigned r; asm("v_med3_u32 %0, %1, %2, %3" : "=v"(r) : "v"(a), "v"(b), "v"(c)); return r; }
#define S3(a, b, c) { const unsigned x_ = ix[a], y_ = ix[b], z_ = ix[c]; ix[a] = min(min(y_, z_), x_); ix[b] = umed3(x_, y_, z_); ix[c] = max(max(y_, z_), x_); }
#define CE(a, b) { const unsigned lo_ = min(ix[a], ix[b]), hi_ = max(ix[a], ix[b]); ix[a] = lo_; ix[b] = hi_; }

struct Acc { f32x4 s0, s1; float q; };

__device__ __forceinline__ void consume(Acc& a, const u32x4 v, const s16x4 ident) {
    const u32x2 lo = {v[0], v[1]}, hi = {v[2], v[3]};
    a.s0 = __builtin_amdgcn_mfma_f32_4x4x4bf16_1k(ident, __builtin_bit_cast(s16x4, lo), a.s0, 0, 0, 0);
    a.s1 = __builtin_amdgcn_mfma_f32_4x4x4bf16_1k(ident, __builtin_bit_cast(s16x4, hi), a.s1, 0, 0, 0);
#pragma unroll
    for (int k = 0; k < 4; ++k) {
        const unsigned w = v[k];
        const bf16x2 h = __builtin_bit_cast(bf16x2, w);
        a.q = __builtin_amdgcn_fdot2_f32_bf16(h, h, a.q, false);
    }
    asm volatile("" : "+v"(a.q));
}

__device__ __forceinline__ float red8(float r) {
    r += __builtin_bit_cast(float, __builtin_amdgcn_update_dpp(0, __builtin_bit_cast(int, r), 0xB1, 0xf, 0xf, true));
    r += __builtin_bit_cast(float, __builtin_amdgcn_update_dpp(0, __builtin_bit_cast(int, r), 0x4E, 0xf, 0xf, true));
    r += __builtin_bit_cast(float, __builtin_amdgcn_update_dpp(0, __builtin_bit_cast(int, r), 0x141, 0xf, 0xf, true));
    return r;
}

template <int k>
__device__ __forceinline__ void round_(u32x4 (&v)[F], Acc& a, const __amdgpu_buffer_rsrc_t rt, const unsigned (&ix)[13], unsigned loff, s16x4 ident) {
    if constexpr (k < F) {
        v[k] = __builtin_amdgcn_raw_buffer_load_b128(rt, bcast_slot<k>(ix) | loff, 0, 0);
        __builtin_amdgcn_sched_barrier(0);
        if constexpr (k % 2 == 1 && k >= B0 && k < F - 1) {
            __builtin_amdgcn_s_barrier();
            __builtin_amdgcn_sched_barrier(0);
        }
    }
    if constexpr (k >= W) { consume(a, v[k - W], ident); __builtin_amdgcn_sched_barrier(0); }
}
template <int K0, int... Is>
__device__ __forceinline__ void rounds(u32x4 (&v)[F], Acc& a, const __amdgpu_buffer_rsrc_t rt, const unsigned (&ix)[13], unsigned loff, s16x4 ident, std::integer_sequence<int, Is...>) {
    (round_<K0 + Is>(v, a, rt, ix, loff, ident), ...);
}

__global__ __launch_bounds__(1024) __attribute__((amdgpu_waves_per_eu(4, 4)))
void fm_kernel(const int* __restrict__ idx, const char* __restrict__ table, unsigned short* __restrict__ out, u64* __restrict__ xch, unsigned table_bytes) {
    asm volatile("" :: "s"(idx), "s"(table), "s"(out), "s"(xch), "s"(table_bytes));
    const int x = blockIdx.x & 7, lb = blockIdx.x >> 3;
    const int cg = (x & 1) ^ 1, sg = x >> 1;
    const unsigned b = (sg * 32 + lb) * 128 + threadIdx.x / 8;
    const unsigned l = threadIdx.x & 7, j = threadIdx.x & 3;
    unsigned loff = cg * 128 + l * 16;
    const unsigned ioff = (b * F + 6 * j + (j ? 1 : 0)) * 4;
    const u32x4 a0 = *(const u32x4*)((const char*)idx + ioff);
    const u32x3 a1 = *(const u32x3*)((const char*)idx + ioff + 16);
    const __amdgpu_buffer_rsrc_t rt = __builtin_amdgcn_make_buffer_rsrc((void*)table, 0, table_bytes, 0x00020000);
    u64 idn = (u64)0x3F80 << (16 * j);
    unsigned xo = (b >> 3) * 16 + (l == 0 ? (b & 7) : 8);
    asm volatile("" : "+v"(loff), "+v"(idn), "+v"(xo));
    const s16x4 ident = __builtin_bit_cast(s16x4, idn);
    __builtin_amdgcn_sched_barrier(0);
    unsigned ix[13];
    ix[0] = a0[0] << 8; ix[1] = a0[1] << 8; ix[2] = a0[2] << 8; ix[3] = a0[3] << 8; ix[4] = a1[0] << 8; ix[5] = a1[1] << 8; ix[6] = a1[2] << 8;
#pragma unroll
    for (int k = 1; k < 7; ++k) ix[6 + k] = (unsigned)__builtin_amdgcn_update_dpp(0, (int)ix[k], 0xEE, 0xf, 0xf, true);
    u32x4 v[F];
    Acc a; a.s0 = a.s1 = f32x4{0.f, 0.f, 0.f, 0.f}; a.q = 0.f;
    {
        unsigned mn[13];
        mn[0] = min(min(min(min(ix[4], ix[5]), ix[3]), min(min(ix[7], ix[8]), ix[6])), min(min(ix[1], ix[2]), ix[0]));
        rounds<0>(v, a, rt, mn, loff, ident, std::make_integer_sequence<int, 2>{});
    }
    S3(0,1,2) S3(3,4,5) S3(6,7,8) S3(9,10,11) S3(0,3,6) S3(7,4,12) S3(9,7,6)
    rounds<2>(v, a, rt, ix, loff, ident, std::make_integer_sequence<int, 2>{});
    S3(1,4,10) S3(8,5,11) S3(8,12,2) S3(6,4,8) S3(10,12,5) S3(3,7,1) S3(5,2,11) CE(8,10) CE(1,6)
    __builtin_amdgcn_sched_barrier(0);
    rounds<4>(v, a, rt, ix, loff, ident, std::make_integer_sequence<int, KE - 4>{});
    u64 old = __hip_atomic_load(xch + xo, __ATOMIC_RELAXED, __HIP_MEMORY_SCOPE_AGENT);
    __builtin_amdgcn_sched_barrier(0);
    rounds<KE>(v, a, rt, ix, loff, ident, std::make_integer_sequence<int, F + W - KE>{});
    float r = -a.q;
#pragma unroll
    for (int k = 0; k < 4; ++k) { r = fmaf(a.s0[k], a.s0[k], r); r = fmaf(a.s1[k], a.s1[k], r); }
    r = red8(r);
    const unsigned ep1 = __builtin_amdgcn_readlane((unsigned)old, 1) + 1u;
    if (l == 0) {
        const unsigned tag = ep1 ^ 0xA5C3E187u;
        const unsigned tlo = (ep1 * 0x6Du + 0x3Bu) & 0xFFu;
        const unsigned mine = __float_as_uint(r) & ~0xFFu;
        bool ok = (unsigned)(old >> 32) == tag && ((unsigned)old & 0xFFu) == tlo;
        if (!ok) {
            old = __hip_atomic_exchange(xch + xo, ((u64)tag << 32) | mine | tlo, __ATOMIC_RELAXED, __HIP_MEMORY_SCOPE_AGENT);
            ok = (unsigned)(old >> 32) == tag && ((unsigned)old & 0xFFu) == tlo;
        }
        if (ok) {
            const __bf16 o = (__bf16)(0.5f * (__uint_as_float(mine) + __uint_as_float((unsigned)old & ~0xFFu)));
            out[b] = __builtin_bit_cast(unsigned short, o);
            if ((threadIdx.x & 63) == 0) __hip_atomic_store((unsigned*)(xch + xo + 8), ep1, __ATOMIC_RELAXED, __HIP_MEMORY_SCOPE_AGENT);
        }
    }
}

extern "C" void kernel_launch(void* const* d_in, const int* in_sizes, int n_in,
                              void* d_out, int out_size, void* d_ws, size_t ws_size,
                              hipStream_t stream) {
    fm_kernel<<<256, 1024, 0, stream>>>((const int*)d_in[0], (const char*)d_in[1], (unsigned short*)d_out, (u64*)d_ws,
                                        (unsigned)in_sizes[1] * 2u);
}
```

```cpp
#include <hip/hip_runtime.h>
#include <stdint.h>
#include <utility>

#define TLBW 0
#define CPF 0
#define B1 18
#define MAPX 2
#define REV 1
#define B0 3
#define KE 26
#define WW 8

typedef unsigned int u32x4 __attribute__((ext_vector_type(4)));
typedef unsigned int u32x3 __attribute__((ext_vector_type(3)));
typedef unsigned int u32x2 __attribute__((ext_vector_type(2)));
typedef float f32x4 __attribute__((ext_vector_type(4)));
typedef short s16x4 __attribute__((ext_vector_type(4)));
typedef __bf16 bf16x2 __attribute__((ext_vector_type(2)));
typedef unsigned long long u64;

constexpr int F = 26;
constexpr int NB = 16384;
constexpr int W = WW;

__host__ __device__ constexpr int slot_r(int k) { constexpr int t[13] = {0, 9, 3, 7, 1, 6, 4, 8, 10, 12, 5, 2, 11}; return t[k >> 1]; }

template <int k>
__device__ __forceinline__ unsigned bcast_slot(const unsigned (&ix)[13]) {
    return (unsigned)__builtin_amdgcn_update_dpp(0, (int)ix[slot_r(k)], (k & 1) ? 0x55 : 0x00, 0xf, 0xf, false);
}
__device__ __forceinline__ unsigned umed3(unsigned a, unsigned b, unsigned c) { unsigned r; asm("v_med3_u32 %0, %1, %2, %3" : "=v"(r) : "v"(a), "v"(b), "v"(c)); return r; }
#define S3(a, b, c) { const unsigned x_ = ix[a], y_ = ix[b], z_ = ix[c]; ix[a] = min(min(y_, z_), x_); ix[b] = umed3(x_, y_, z_); ix[c] = max(max(y_, z_), x_); }
#define CE(a, b) { const unsigned lo_ = min(ix[a], ix[b]), hi_ = max(ix[a], ix[b]); ix[a] = lo_; ix[b] = hi_; }

struct Acc { f32x4 s0, s1; float q; };

__device__ __forceinline__ void consume(Acc& a, const u32x4 v, const s16x4 ident) {
    const u32x2 lo = {v[0], v[1]}, hi = {v[2], v[3]};
    a.s0 = __builtin_amdgcn_mfma_f32_4x4x4bf16_1k(ident, __builtin_bit_cast(s16x4, lo), a.s0, 0, 0, 0);
    a.s1 = __builtin_amdgcn_mfma_f32_4x4x4bf16_1k(ident, __builtin_bit_cast(s16x4, hi), a.s1, 0, 0, 0);
#pragma unroll
    for (int k = 0; k < 4; ++k) {
        const unsigned w = v[k];
        const bf16x2 h = __builtin_bit_cast(bf16x2, w);
        a.q = __builtin_amdgcn_fdot2_f32_bf16(h, h, a.q, false);
    }
    asm volatile("" : "+v"(a.q));
}

__device__ __forceinline__ float red8(float r) {
    r += __builtin_bit_cast(float, __builtin_amdgcn_update_dpp(0, __builtin_bit_cast(int, r), 0xB1, 0xf, 0xf, true));
    r += __builtin_bit_cast(float, __builtin_amdgcn_update_dpp(0, __builtin_bit_cast(int, r), 0x4E, 0xf, 0xf, true));
    r += __builtin_bit_cast(float, __builtin_amdgcn_update_dpp(0, __builtin_bit_cast(int, r), 0x141, 0xf, 0xf, true));
    return r;
}

template <int k>
__device__ __forceinline__ void round_(u32x4 (&v)[F], Acc& a, const __amdgpu_buffer_rsrc_t rt, const unsigned (&ix)[13], unsigned loff, s16x4 ident) {
    if constexpr (k < F) {
        v[k] = __builtin_amdgcn_raw_buffer_load_b128(rt, bcast_slot<k>(ix) | loff, 0, 0);
        __builtin_amdgcn_sched_barrier(0);
        if constexpr (k % 2 == 1 && k >= B0 && k <= B1) {
            __builtin_amdgcn_s_barrier();
            __builtin_amdgcn_sched_barrier(0);
        }
    }
    if constexpr (k >= W) { consume(a, v[k - W], ident); __builtin_amdgcn_sched_barrier(0); }
}
template <int K0, int... Is>
__device__ __forceinline__ void rounds(u32x4 (&v)[F], Acc& a, const __amdgpu_buffer_rsrc_t rt, const unsigned (&ix)[13], unsigned loff, s16x4 ident, std::integer_sequence<int, Is...>) {
    (round_<K0 + Is>(v, a, rt, ix, loff, ident), ...);
}

__global__ __launch_bounds__(1024) __attribute__((amdgpu_waves_per_eu(4, 4)))
void fm_kernel(const int* __restrict__ idx, const char* __restrict__ table, unsigned short* __restrict__ out, u64* __restrict__ xch, unsigned table_bytes) {
    asm volatile("" :: "s"(idx), "s"(table), "s"(out), "s"(xch), "s"(table_bytes));
    const int x = blockIdx.x & 7, lb = blockIdx.x >> 3;
    const int cg = MAPX == 2 ? (x >> 2) : (x & 1) ^ MAPX, sg = MAPX == 2 ? (x & 3) : x >> 1;
    const unsigned b = (sg * 32 + lb) * 128 + ((threadIdx.x / 8) ^ (REV && cg ? 120 : 0));
    const unsigned l = threadIdx.x & 7, j = threadIdx.x & 3;
    unsigned loff = cg * 128 + l * 16;
    const unsigned ioff = (b * F + 6 * j + (j ? 1 : 0)) * 4;
    const u32x4 a0 = *(const u32x4*)((const char*)idx + ioff);
    const u32x3 a1 = *(const u32x3*)((const char*)idx + ioff + 16);
    const __amdgpu_buffer_rsrc_t rt = __builtin_amdgcn_make_buffer_rsrc((void*)table, 0, table_bytes, 0x00020000);
    u64 idn = (u64)0x3F80 << (16 * j);
    unsigned xo = (b >> 3) * 16 + (l == 0 ? (b & 7) : 8);
    asm volatile("" : "+v"(loff), "+v"(idn), "+v"(xo));
    const s16x4 ident = __builtin_bit_cast(s16x4, idn);
    __builtin_amdgcn_sched_barrier(0);
    unsigned ix[13];
    ix[0] = a0[0] << 8; ix[1] = a0[1] << 8; ix[2] = a0[2] << 8; ix[3] = a0[3] << 8; ix[4] = a1[0] << 8; ix[5] = a1[1] << 8; ix[6] = a1[2] << 8;
#pragma unroll
    for (int k = 1; k < 7; ++k) ix[6 + k] = (unsigned)__builtin_amdgcn_update_dpp(0, (int)ix[k], 0xEE, 0xf, 0xf, true);
    u32x4 v[F];
    Acc a; a.s0 = a.s1 = f32x4{0.f, 0.f, 0.f, 0.f}; a.q = 0.f;
    {
        unsigned mn[13];
        mn[0] = min(min(min(min(ix[4], ix[5]), ix[3]), min(min(ix[7], ix[8]), ix[6])), min(min(ix[1], ix[2]), ix[0]));
        rounds<0>(v, a, rt, mn, loff, ident, std::make_integer_sequence<int, 2>{});
    }
    S3(0,1,2) S3(3,4,5) S3(6,7,8) S3(9,10,11) S3(0,3,6) S3(7,4,12) S3(9,7,6)
    rounds<2>(v, a, rt, ix, loff, ident, std::make_integer_sequence<int, 2>{});
    S3(1,4,10) S3(8,5,11) S3(8,12,2) S3(6,4,8) S3(10,12,5) S3(3,7,1) S3(5,2,11) CE(8,10) CE(1,6)
    __builtin_amdgcn_sched_barrier(0);
    rounds<4>(v, a, rt, ix, loff, ident, std::make_integer_sequence<int, KE - 4>{});
    u64 old = __hip_atomic_load(xch + xo, __ATOMIC_RELAXED, __HIP_MEMORY_SCOPE_AGENT);
    __builtin_amdgcn_sched_barrier(0);
    rounds<KE>(v, a, rt, ix, loff, ident, std::make_integer_sequence<int, F + W - KE>{});
    float r = -a.q;
#pragma unroll
    for (int k = 0; k < 4; ++k) { r = fmaf(a.s0[k], a.s0[k], r); r = fmaf(a.s1[k], a.s1[k], r); }
    r = red8(r);
    const unsigned ep1 = __builtin_amdgcn_readlane((unsigned)old, 1) + 1u;
    if (l == 0) {
        const unsigned tag = ep1 ^ 0xA5C3E187u;
        const unsigned tlo = (ep1 * 0x6Du + 0x3Bu) & 0xFFu;
        const unsigned mine = __float_as_uint(r) & ~0xFFu;
        bool ok = (unsigned)(old >> 32) == tag && ((unsigned)old & 0xFFu) == tlo;
        if (!ok) {
            old = __hip_atomic_exchange(xch + xo, ((u64)tag << 32) | mine | tlo, __ATOMIC_RELAXED, __HIP_MEMORY_SCOPE_AGENT);
            ok = (unsigned)(old >> 32) == tag && ((unsigned)old & 0xFFu) == tlo;
        }
        if (ok) {
            const __bf16 o = (__bf16)(0.5f * (__uint_as_float(mine) + __uint_as_float((unsigned)old & ~0xFFu)));
            out[b] = __builtin_bit_cast(unsigned short, o);
            if ((threadIdx.x & 63) == 0) __hip_atomic_store((unsigned*)(xch + xo + 8), ep1, __ATOMIC_RELAXED, __HIP_MEMORY_SCOPE_AGENT);
        }
    }
}

extern "C" void kernel_launch(void* const* d_in, const int* in_sizes, int n_in,
                              void* d_out, int out_size, void* d_ws, size_t ws_size,
                              hipStream_t stream) {
    fm_kernel<<<256, 1024, 0, stream>>>((const int*)d_in[0], (const char*)d_in[1], (unsigned short*)d_out, (u64*)d_ws,
                                        (unsigned)in_sizes[1] * 2u);
}
```

```cpp
#include <hip/hip_runtime.h>
#include <stdint.h>
#include <utility>

typedef unsigned int u32x4 __attribute__((ext_vector_type(4)));
typedef unsigned int u32x2 __attribute__((ext_vector_type(2)));
typedef float f32x4 __attribute__((ext_vector_type(4)));
typedef short s16x4 __attribute__((ext_vector_type(4)));
typedef __bf16 bf16x2 __attribute__((ext_vector_type(2)));
typedef unsigned long long u64;

constexpr int F = 26;
constexpr int W = 8;

__host__ __device__ constexpr int slot_r(int k) { constexpr int t[13] = {0, 9, 3, 7, 1, 6, 4, 8, 10, 12, 5, 2, 11}; return t[k >> 1]; }

template <int k>
__device__ __forceinline__ unsigned bcast_slot(const unsigned (&ix)[13]) {
    return (unsigned)__builtin_amdgcn_update_dpp(0, (int)ix[slot_r(k)], (k & 1) ? 0x55 : 0x00, 0xf, 0xf, false);
}
__device__ __forceinline__ unsigned umed3(unsigned a, unsigned b, unsigned c) {
    unsigned r;
    asm("v_med3_u32 %0, %1, %2, %3" : "=v"(r) : "v"(a), "v"(b), "v"(c));
    return r;
}
#define S3(a, b, c) { const unsigned x_ = ix[a], y_ = ix[b], z_ = ix[c]; ix[a] = min(min(y_, z_), x_); ix[b] = umed3(x_, y_, z_); ix[c] = max(max(y_, z_), x_); }
#define CE(a, b) { const unsigned lo_ = min(ix[a], ix[b]), hi_ = max(ix[a], ix[b]); ix[a] = lo_; ix[b] = hi_; }

struct Acc { f32x4 s0, s1; float q; };

__device__ __forceinline__ void consume(Acc& a, const u32x4 v, const s16x4 ident) {
    const u32x2 lo = {v[0], v[1]}, hi = {v[2], v[3]};
    a.s0 = __builtin_amdgcn_mfma_f32_4x4x4bf16_1k(ident, __builtin_bit_cast(s16x4, lo), a.s0, 0, 0, 0);
    a.s1 = __builtin_amdgcn_mfma_f32_4x4x4bf16_1k(ident, __builtin_bit_cast(s16x4, hi), a.s1, 0, 0, 0);
#pragma unroll
    for (int k = 0; k < 4; ++k) {
        const unsigned w = v[k];
        const bf16x2 h = __builtin_bit_cast(bf16x2, w);
        a.q = __builtin_amdgcn_fdot2_f32_bf16(h, h, a.q, false);
    }
    asm volatile("" : "+v"(a.q));
}

__device__ __forceinline__ float red8(float r) {
    r += __builtin_bit_cast(float, __builtin_amdgcn_update_dpp(0, __builtin_bit_cast(int, r), 0xB1, 0xf, 0xf, true));
    r += __builtin_bit_cast(float, __builtin_amdgcn_update_dpp(0, __builtin_bit_cast(int, r), 0x4E, 0xf, 0xf, true));
    r += __builtin_bit_cast(float, __builtin_amdgcn_update_dpp(0, __builtin_bit_cast(int, r), 0x141, 0xf, 0xf, true));
    return r;
}

template <int k>
__device__ __forceinline__ void round_(u32x4 (&v)[F], Acc& a, const __amdgpu_buffer_rsrc_t rt, const unsigned (&ix)[13], unsigned loff, s16x4 ident) {
    if constexpr (k < F) {
        v[k] = __builtin_amdgcn_raw_buffer_load_b128(rt, bcast_slot<k>(ix) | loff, 0, 0);
        __builtin_amdgcn_sched_barrier(0);
        if constexpr (k % 2 == 1 && k >= 3 && k <= 18) {
            __builtin_amdgcn_s_barrier();
            __builtin_amdgcn_sched_barrier(0);
        }
    }
    if constexpr (k >= W) { consume(a, v[k - W], ident); __builtin_amdgcn_sched_barrier(0); }
}
template <int K0, int... Is>
__device__ __forceinline__ void rounds(u32x4 (&v)[F], Acc& a, const __amdgpu_buffer_rsrc_t rt, const unsigned (&ix)[13], unsigned loff, s16x4 ident, std::integer_sequence<int, Is...>) {
    (round_<K0 + Is>(v, a, rt, ix, loff, ident), ...);
}

__global__ __launch_bounds__(1024) __attribute__((amdgpu_waves_per_eu(4, 4)))
void fm_kernel(const int* __restrict__ idx, const char* __restrict__ table, unsigned short* __restrict__ out, u64* __restrict__ xch, unsigned table_bytes) {
    asm volatile("" :: "s"(idx), "s"(table), "s"(out), "s"(xch), "s"(table_bytes));
    const int x = blockIdx.x & 7, lb = blockIdx.x >> 3;
    const int cg = x >> 2, sg = x & 3;
    const unsigned b = (sg * 32 + lb) * 128 + ((threadIdx.x / 8) ^ (cg ? 120 : 0));
    const unsigned l = threadIdx.x & 7, j = threadIdx.x & 3;
    unsigned loff = cg * 128 + l * 16;
    const unsigned ioff = (b * F + 6 * j + (j ? 1 : 0)) * 4;
    const unsigned* ip = (const unsigned*)((const char*)idx + ioff);
    const unsigned i0 = ip[0], i1 = ip[1], i2 = ip[2], i3 = ip[3], i4 = ip[4], i5 = ip[5], i6 = ip[6];
    const __amdgpu_buffer_rsrc_t rt = __builtin_amdgcn_make_buffer_rsrc((void*)table, 0, table_bytes, 0x00020000);
    u64 idn = (u64)0x3F80 << (16 * j);
    unsigned xo = (b >> 3) * 16 + (l == 0 ? (b & 7) : 8);
    asm volatile("" : "+v"(loff), "+v"(idn), "+v"(xo));
    const s16x4 ident = __builtin_bit_cast(s16x4, idn);
    __builtin_amdgcn_sched_barrier(0);

    unsigned ix[13];
    ix[0] = i0 << 8; ix[1] = i1 << 8; ix[2] = i2 << 8; ix[3] = i3 << 8; ix[4] = i4 << 8; ix[5] = i5 << 8; ix[6] = i6 << 8;
#pragma unroll
    for (int k = 1; k < 7; ++k) ix[6 + k] = (unsigned)__builtin_amdgcn_update_dpp(0, (int)ix[k], 0xEE, 0xf, 0xf, true);

    u32x4 v[F];
    Acc a; a.s0 = a.s1 = f32x4{0.f, 0.f, 0.f, 0.f}; a.q = 0.f;
    {
        unsigned mn[13];
        mn[0] = min(min(min(min(ix[4], ix[5]), ix[3]), min(min(ix[7], ix[8]), ix[6])), min(min(ix[1], ix[2]), ix[0]));
        rounds<0>(v, a, rt, mn, loff, ident, std::make_integer_sequence<int, 2>{});
    }
    S3(0,1,2) S3(3,4,5) S3(6,7,8) S3(9,10,11) S3(0,3,6) S3(7,4,12) S3(9,7,6)
    rounds<2>(v, a, rt, ix, loff, ident, std::make_integer_sequence<int, 2>{});
    S3(1,4,10) S3(8,5,11) S3(8,12,2) S3(6,4,8) S3(10,12,5) S3(3,7,1) S3(5,2,11) CE(8,10) CE(1,6)
    __builtin_amdgcn_sched_barrier(0);
    rounds<4>(v, a, rt, ix, loff, ident, std::make_integer_sequence<int, F - 4>{});
    u64 old = __hip_atomic_load(xch + xo, __ATOMIC_RELAXED, __HIP_MEMORY_SCOPE_AGENT);
    __builtin_amdgcn_sched_barrier(0);
    rounds<F>(v, a, rt, ix, loff, ident, std::make_integer_sequence<int, W>{});

    float r = -a.q;
#pragma unroll
    for (int k = 0; k < 4; ++k) { r = fmaf(a.s0[k], a.s0[k], r); r = fmaf(a.s1[k], a.s1[k], r); }
    r = red8(r);

    const unsigned ep1 = __builtin_amdgcn_readlane((unsigned)old, 1) + 1u;
    if (l == 0) {
        const unsigned tag = ep1 ^ 0xA5C3E187u;
        const unsigned tlo = (ep1 * 0x6Du + 0x3Bu) & 0xFFu;
        const unsigned mine = __float_as_uint(r) & ~0xFFu;
        bool ok = (unsigned)(old >> 32) == tag && ((unsigned)old & 0xFFu) == tlo;
        if (!ok) {
            old = __hip_atomic_exchange(xch + xo, ((u64)tag << 32) | mine | tlo, __ATOMIC_RELAXED, __HIP_MEMORY_SCOPE_AGENT);
            ok = (unsigned)(old >> 32) == tag && ((unsigned)old & 0xFFu) == tlo;
        }
        if (ok) {
            const __bf16 o = (__bf16)(0.5f * (__uint_as_float(mine) + __uint_as_float((unsigned)old & ~0xFFu)));
            __hip_atomic_store(out + b, __builtin_bit_cast(unsigned short, o), __ATOMIC_RELAXED, __HIP_MEMORY_SCOPE_AGENT);
            if ((threadIdx.x & 63) == 0) __hip_atomic_store((unsigned*)(xch + xo + 8), ep1, __ATOMIC_RELAXED, __HIP_MEMORY_SCOPE_AGENT);
        }
    }
}

extern "C" void kernel_launch(void* const* d_in, const int* in_sizes, int n_in,
                              void* d_out, int out_size, void* d_ws, size_t ws_size,
                              hipStream_t stream) {
    fm_kernel<<<256, 1024, 0, stream>>>((const int*)d_in[0], (const char*)d_in[1], (unsigned short*)d_out, (u64*)d_ws,
                                        (unsigned)in_sizes[1] * 2u);
}
```

```cpp
#include <hip/hip_runtime.h>
#include <stdint.h>
#include <utility>

typedef unsigned int u32x4 __attribute__((ext_vector_type(4)));
typedef unsigned int u32x2 __attribute__((ext_vector_type(2)));
typedef float f32x4 __attribute__((ext_vector_type(4)));
typedef short s16x4 __attribute__((ext_vector_type(4)));
typedef __bf16 bf16x2 __attribute__((ext_vector_type(2)));
typedef unsigned long long u64;

constexpr int F = 26;
constexpr int W = 8;

__host__ __device__ constexpr int slot_r(int k) { constexpr int t[13] = {0, 9, 3, 7, 1, 6, 4, 8, 10, 12, 5, 2, 11}; return t[k >> 1]; }

template <int k>
__device__ __forceinline__ unsigned bcast_slot(const unsigned (&ix)[13]) {
    return (unsigned)__builtin_amdgcn_update_dpp(0, (int)ix[slot_r(k)], (k & 1) ? 0x55 : 0x00, 0xf, 0xf, false);
}
__device__ __forceinline__ unsigned umed3(unsigned a, unsigned b, unsigned c) {
    unsigned r;
    asm("v_med3_u32 %0, %1, %2, %3" : "=v"(r) : "v"(a), "v"(b), "v"(c));
    return r;
}
#define S3(a, b, c) { const unsigned x_ = ix[a], y_ = ix[b], z_ = ix[c]; ix[a] = min(min(y_, z_), x_); ix[b] = umed3(x_, y_, z_); ix[c] = max(max(y_, z_), x_); }
#define CE(a, b) { const unsigned lo_ = min(ix[a], ix[b]), hi_ = max(ix[a], ix[b]); ix[a] = lo_; ix[b] = hi_; }

struct Acc { f32x4 s0, s1; float q; };

__device__ __forceinline__ void consume(Acc& a, const u32x4 v, const s16x4 ident) {
    const u32x2 lo = {v[0], v[1]}, hi = {v[2], v[3]};
    a.s0 = __builtin_amdgcn_mfma_f32_4x4x4bf16_1k(ident, __builtin_bit_cast(s16x4, lo), a.s0, 0, 0, 0);
    a.s1 = __builtin_amdgcn_mfma_f32_4x4x4bf16_1k(ident, __builtin_bit_cast(s16x4, hi), a.s1, 0, 0, 0);
#pragma unroll
    for (int k = 0; k < 4; ++k) {
        const unsigned w = v[k];
        const bf16x2 h = __builtin_bit_cast(bf16x2, w);
        a.q = __builtin_amdgcn_fdot2_f32_bf16(h, h, a.q, false);
    }
    asm volatile("" : "+v"(a.q));
}

__device__ __forceinline__ float red8(float r) {
    r += __builtin_bit_cast(float, __builtin_amdgcn_update_dpp(0, __builtin_bit_cast(int, r), 0xB1, 0xf, 0xf, true));
    r += __builtin_bit_cast(float, __builtin_amdgcn_update_dpp(0, __builtin_bit_cast(int, r), 0x4E, 0xf, 0xf, true));
    r += __builtin_bit_cast(float, __builtin_amdgcn_update_dpp(0, __builtin_bit_cast(int, r), 0x141, 0xf, 0xf, true));
    return r;
}

template <int k>
__device__ __forceinline__ void round_(u32x4 (&v)[F], Acc& a, const __amdgpu_buffer_rsrc_t rt, const unsigned (&ix)[13], unsigned loff, s16x4 ident) {
    if constexpr (k < F) {
        v[k] = __builtin_amdgcn_raw_buffer_load_b128(rt, bcast_slot<k>(ix) | loff, 0, 0);
        __builtin_amdgcn_sched_barrier(0);
        if constexpr (k % 2 == 1 && k >= 3 && k <= 18) {
            __builtin_amdgcn_s_barrier();
            __builtin_amdgcn_sched_barrier(0);
        }
    }
    if constexpr (k >= W) { consume(a, v[k - W], ident); __builtin_amdgcn_sched_barrier(0); }
}
template <int K0, int... Is>
__device__ __forceinline__ void rounds(u32x4 (&v)[F], Acc& a, const __amdgpu_buffer_rsrc_t rt, const unsigned (&ix)[13], unsigned loff, s16x4 ident, std::integer_sequence<int, Is...>) {
    (round_<K0 + Is>(v, a, rt, ix, loff, ident), ...);
}

__global__ __launch_bounds__(1024) __attribute__((amdgpu_waves_per_eu(4, 4)))
void fm_kernel(const int* __restrict__ idx, const char* __restrict__ table, unsigned short* __restrict__ out, u64* __restrict__ xch, unsigned table_bytes) {
    u64 tz_ = __builtin_amdgcn_s_memrealtime(); u64 tq_ = 0; __builtin_amdgcn_sched_barrier(0);
    asm volatile("" :: "s"(idx), "s"(table), "s"(out), "s"(xch), "s"(table_bytes));
    __builtin_amdgcn_sched_barrier(0); tq_ = __builtin_amdgcn_s_memrealtime(); __builtin_amdgcn_sched_barrier(0);
    const int x = blockIdx.x & 7, lb = blockIdx.x >> 3;
    const int cg = x >> 2, sg = x & 3;
    const unsigned b = (sg * 32 + lb) * 128 + ((threadIdx.x / 8) ^ (cg ? 120 : 0));
    const unsigned l = threadIdx.x & 7, j = threadIdx.x & 3;
    unsigned loff = cg * 128 + l * 16;
    const unsigned ioff = (b * F + 6 * j + (j ? 1 : 0)) * 4;
    const unsigned* ip = (const unsigned*)((const char*)idx + ioff);
    const unsigned i0 = ip[0], i1 = ip[1], i2 = ip[2], i3 = ip[3], i4 = ip[4], i5 = ip[5], i6 = ip[6];
    const __amdgpu_buffer_rsrc_t rt = __builtin_amdgcn_make_buffer_rsrc((void*)table, 0, table_bytes, 0x00020000);
    u64 idn = (u64)0x3F80 << (16 * j);
    unsigned xo = (b >> 3) * 16 + (l == 0 ? (b & 7) : 8);
    asm volatile("" : "+v"(loff), "+v"(idn), "+v"(xo));
    const s16x4 ident = __builtin_bit_cast(s16x4, idn);
    __builtin_amdgcn_sched_barrier(0);

    unsigned ix[13];
    ix[0] = i0 << 8; ix[1] = i1 << 8; ix[2] = i2 << 8; ix[3] = i3 << 8; ix[4] = i4 << 8; ix[5] = i5 << 8; ix[6] = i6 << 8;
#pragma unroll
    for (int k = 1; k < 7; ++k) ix[6 + k] = (unsigned)__builtin_amdgcn_update_dpp(0, (int)ix[k], 0xEE, 0xf, 0xf, true);

    asm volatile("" :: "v"(ix[0]), "v"(ix[12]));
    u32x4 v[F];
    Acc a; a.s0 = a.s1 = f32x4{0.f, 0.f, 0.f, 0.f}; a.q = 0.f;
    {
        unsigned mn[13];
        mn[0] = min(min(min(min(ix[4], ix[5]), ix[3]), min(min(ix[7], ix[8]), ix[6])), min(min(ix[1], ix[2]), ix[0]));
        rounds<0>(v, a, rt, mn, loff, ident, std::make_integer_sequence<int, 2>{});
    }
    S3(0,1,2) S3(3,4,5) S3(6,7,8) S3(9,10,11) S3(0,3,6) S3(7,4,12) S3(9,7,6)
    rounds<2>(v, a, rt, ix, loff, ident, std::make_integer_sequence<int, 2>{});
    S3(1,4,10) S3(8,5,11) S3(8,12,2) S3(6,4,8) S3(10,12,5) S3(3,7,1) S3(5,2,11) CE(8,10) CE(1,6)
    __builtin_amdgcn_sched_barrier(0);
    rounds<4>(v, a, rt, ix, loff, ident, std::make_integer_sequence<int, F - 4>{});
    u64 old = __hip_atomic_load(xch + xo, __ATOMIC_RELAXED, __HIP_MEMORY_SCOPE_AGENT);
    __builtin_amdgcn_sched_barrier(0);
    rounds<F>(v, a, rt, ix, loff, ident, std::make_integer_sequence<int, W>{});

    float r = -a.q;
#pragma unroll
    for (int k = 0; k < 4; ++k) { r = fmaf(a.s0[k], a.s0[k], r); r = fmaf(a.s1[k], a.s1[k], r); }
    r = red8(r);

    const unsigned ep1 = __builtin_amdgcn_readlane((unsigned)old, 1) + 1u;
    if (l == 0) {
        const unsigned tag = ep1 ^ 0xA5C3E187u;
        const unsigned tlo = (ep1 * 0x6Du + 0x3Bu) & 0xFFu;
        const unsigned mine = __float_as_uint(r) & ~0xFFu;
        bool ok = (unsigned)(old >> 32) == tag && ((unsigned)old & 0xFFu) == tlo;
        if (!ok) {
            old = __hip_atomic_exchange(xch + xo, ((u64)tag << 32) | mine | tlo, __ATOMIC_RELAXED, __HIP_MEMORY_SCOPE_AGENT);
            ok = (unsigned)(old >> 32) == tag && ((unsigned)old & 0xFFu) == tlo;
        }
        if (ok) {
            const __bf16 o = (__bf16)(0.5f * (__uint_as_float(mine) + __uint_as_float((unsigned)old & ~0xFFu)));
            __hip_atomic_store(out + b, __builtin_bit_cast(unsigned short, o), __ATOMIC_RELAXED, __HIP_MEMORY_SCOPE_AGENT);
            if ((threadIdx.x & 63) == 0) __hip_atomic_store((unsigned*)(xch + xo + 8), ep1, __ATOMIC_RELAXED, __HIP_MEMORY_SCOPE_AGENT);
        }
    }
    if ((threadIdx.x & 63) == 0) { u64* ts_ = xch + (8 << 20); int w_ = blockIdx.x * 16 + threadIdx.x / 64; ts_[w_] = tz_; ts_[4096 + w_] = tq_; }
}


__global__ __launch_bounds__(1024) void probe_kernel(const u64* __restrict__ ts, int kmul) {
    __shared__ u64 sm[3][16];
    u64 mn0 = ~0ull, mx1 = 0, mn1 = ~0ull, sum = 0;
    for (int i = threadIdx.x; i < 4096; i += 1024) {
        u64 a = ts[i], b = ts[4096 + i];
        mn0 = a < mn0 ? a : mn0; mx1 = b > mx1 ? b : mx1; mn1 = b < mn1 ? b : mn1;
    }
    for (int m = 32; m >= 1; m >>= 1) {
        u64 x;
        x = __shfl_xor(mn0, m); mn0 = x < mn0 ? x : mn0;
        x = __shfl_xor(mx1, m); mx1 = x > mx1 ? x : mx1;
        x = __shfl_xor(mn1, m); mn1 = x < mn1 ? x : mn1;
    }
    int w = threadIdx.x >> 6;
    if ((threadIdx.x & 63) == 0) { sm[0][w] = mn0; sm[1][w] = mx1; sm[2][w] = mn1; }
    __syncthreads();
    if (threadIdx.x == 0) {
        for (int i = 0; i < 16; ++i) { mn0 = sm[0][i] < mn0 ? sm[0][i] : mn0; mx1 = sm[1][i] > mx1 ? sm[1][i] : mx1; mn1 = sm[2][i] < mn1 ? sm[2][i] : mn1; }
        u64 d = (kmul & 0x10000) ? mn1 - mn0 : mx1 - mn0;
        if (d > 10000) d = 10000;
        u64 tstart = __builtin_amdgcn_s_memrealtime();
        while (__builtin_amdgcn_s_memrealtime() - tstart < d * (kmul & 0xffff)) __builtin_amdgcn_s_sleep(1);
    }
}
extern "C" void kernel_launch(void* const* d_in, const int* in_sizes, int n_in,
                              void* d_out, int out_size, void* d_ws, size_t ws_size,
                              hipStream_t stream) {
    fm_kernel<<<256, 1024, 0, stream>>>((const int*)d_in[0], (const char*)d_in[1], (unsigned short*)d_out, (u64*)d_ws,
                                        (unsigned)in_sizes[1] * 2u);
    probe_kernel<<<1, 1024, 0, stream>>>((const u64*)d_ws + (8 << 20), 65556);
}
```

```cpp
#include <hip/hip_runtime.h>
#include <stdint.h>
#include <utility>

typedef unsigned int u32x4 __attribute__((ext_vector_type(4)));
typedef unsigned int u32x2 __attribute__((ext_vector_type(2)));
typedef float f32x4 __attribute__((ext_vector_type(4)));
typedef short s16x4 __attribute__((ext_vector_type(4)));
typedef __bf16 bf16x2 __attribute__((ext_vector_type(2)));
typedef unsigned long long u64;

constexpr int F = 26;
constexpr int W = 8;

__host__ __device__ constexpr int slot_r(int k) { constexpr int t[13] = {0, 9, 3, 7, 1, 6, 4, 8, 10, 12, 5, 2, 11}; return t[k >> 1]; }

template <int k>
__device__ __forceinline__ unsigned bcast_slot(const unsigned (&ix)[13]) {
    return (unsigned)__builtin_amdgcn_update_dpp(0, (int)ix[slot_r(k)], (k & 1) ? 0x55 : 0x00, 0xf, 0xf, false);
}
__device__ __forceinline__ unsigned umed3(unsigned a, unsigned b, unsigned c) {
    unsigned r;
    asm("v_med3_u32 %0, %1, %2, %3" : "=v"(r) : "v"(a), "v"(b), "v"(c));
    return r;
}
#define S3(a, b, c) { const unsigned x_ = ix[a], y_ = ix[b], z_ = ix[c]; ix[a] = min(min(y_, z_), x_); ix[b] = umed3(x_, y_, z_); ix[c] = max(max(y_, z_), x_); }
#define CE(a, b) { const unsigned lo_ = min(ix[a], ix[b]), hi_ = max(ix[a], ix[b]); ix[a] = lo_; ix[b] = hi_; }

struct Acc { f32x4 s0, s1; float q; };

__device__ __forceinline__ void consume(Acc& a, const u32x4 v, const s16x4 ident) {
    const u32x2 lo = {v[0], v[1]}, hi = {v[2], v[3]};
    a.s0 = __builtin_amdgcn_mfma_f32_4x4x4bf16_1k(ident, __builtin_bit_cast(s16x4, lo), a.s0, 0, 0, 0);
    a.s1 = __builtin_amdgcn_mfma_f32_4x4x4bf16_1k(ident, __builtin_bit_cast(s16x4, hi), a.s1, 0, 0, 0);
#pragma unroll
    for (int k = 0; k < 4; ++k) {
        const unsigned w = v[k];
        const bf16x2 h = __builtin_bit_cast(bf16x2, w);
        a.q = __builtin_amdgcn_fdot2_f32_bf16(h, h, a.q, false);
    }
    asm volatile("" : "+v"(a.q));
}

__device__ __forceinline__ float red8(float r) {
    r += __builtin_bit_cast(float, __builtin_amdgcn_update_dpp(0, __builtin_bit_cast(int, r), 0xB1, 0xf, 0xf, true));
    r += __builtin_bit_cast(float, __builtin_amdgcn_update_dpp(0, __builtin_bit_cast(int, r), 0x4E, 0xf, 0xf, true));
    r += __builtin_bit_cast(float, __builtin_amdgcn_update_dpp(0, __builtin_bit_cast(int, r), 0x141, 0xf, 0xf, true));
    return r;
}

template <int k>
__device__ __forceinline__ void round_(u32x4 (&v)[F], Acc& a, const __amdgpu_buffer_rsrc_t rt, const unsigned (&ix)[13], unsigned loff, s16x4 ident) {
    if constexpr (k < F) {
        v[k] = __builtin_amdgcn_raw_buffer_load_b128(rt, bcast_slot<k>(ix) | loff, 0, 0);
        __builtin_amdgcn_sched_barrier(0);
        if constexpr (k % 2 == 1 && k >= 3 && k <= 18) {
            __builtin_amdgcn_s_barrier();
            __builtin_amdgcn_sched_barrier(0);
        }
    }
    if constexpr (k >= W) { consume(a, v[k - W], ident); __builtin_amdgcn_sched_barrier(0); }
}
template <int K0, int... Is>
__device__ __forceinline__ void rounds(u32x4 (&v)[F], Acc& a, const __amdgpu_buffer_rsrc_t rt, const unsigned (&ix)[13], unsigned loff, s16x4 ident, std::integer_sequence<int, Is...>) {
    (round_<K0 + Is>(v, a, rt, ix, loff, ident), ...);
}

__global__ __launch_bounds__(1024) __attribute__((amdgpu_waves_per_eu(4, 4)))
void fm_kernel(const int* __restrict__ idx, const char* __restrict__ table, unsigned short* __restrict__ out, u64* __restrict__ xch, unsigned table_bytes) {
    asm volatile("" :: "s"(idx), "s"(table), "s"(out), "s"(xch), "s"(table_bytes));
    const int x = blockIdx.x & 7, lb = blockIdx.x >> 3;
    const int cg = x >> 2, sg = x & 3;
    const unsigned b = (sg * 32 + lb) * 128 + ((threadIdx.x / 8) ^ (cg ? 120 : 0));
    const unsigned l = threadIdx.x & 7, j = threadIdx.x & 3;
    unsigned loff = cg * 128 + l * 16;
    const unsigned ioff = (b * F + 6 * j + (j ? 1 : 0)) * 4;
    const unsigned* ip = (const unsigned*)((const char*)idx + ioff);
    const unsigned i0 = ip[0], i1 = ip[1], i2 = ip[2], i3 = ip[3], i4 = ip[4], i5 = ip[5], i6 = ip[6];
    const __amdgpu_buffer_rsrc_t rt = __builtin_amdgcn_make_buffer_rsrc((void*)table, 0, table_bytes, 0x00020000);
    u64 idn = (u64)0x3F80 << (16 * j);
    unsigned xo = (b >> 3) * 16 + (l == 0 ? (b & 7) : 8);
    asm volatile("" : "+v"(loff), "+v"(idn), "+v"(xo));
    const s16x4 ident = __builtin_bit_cast(s16x4, idn);
    __builtin_amdgcn_sched_barrier(0);

    unsigned ix[13];
    ix[0] = i0 << 8; ix[1] = i1 << 8; ix[2] = i2 << 8; ix[3] = i3 << 8; ix[4] = i4 << 8; ix[5] = i5 << 8; ix[6] = i6 << 8;
#pragma unroll
    for (int k = 1; k < 7; ++k) ix[6 + k] = (unsigned)__builtin_amdgcn_update_dpp(0, (int)ix[k], 0xEE, 0xf, 0xf, true);

    u32x4 v[F];
    Acc a; a.s0 = a.s1 = f32x4{0.f, 0.f, 0.f, 0.f}; a.q = 0.f;
    {
        unsigned mn[13];
        mn[0] = min(min(min(min(ix[4], ix[5]), ix[3]), min(min(ix[7], ix[8]), ix[6])), min(min(ix[1], ix[2]), ix[0]));
        rounds<0>(v, a, rt, mn, loff, ident, std::make_integer_sequence<int, 2>{});
    }
    S3(0,1,2) S3(3,4,5) S3(6,7,8) S3(9,10,11) S3(0,3,6) S3(7,4,12) S3(9,7,6)
    rounds<2>(v, a, rt, ix, loff, ident, std::make_integer_sequence<int, 2>{});
    S3(1,4,10) S3(8,5,11) S3(8,12,2) S3(6,4,8) S3(10,12,5) S3(3,7,1) S3(5,2,11) CE(8,10) CE(1,6)
    __builtin_amdgcn_sched_barrier(0);
    rounds<4>(v, a, rt, ix, loff, ident, std::make_integer_sequence<int, F - 4>{});
    u64 old = __hip_atomic_load(xch + xo, __ATOMIC_RELAXED, __HIP_MEMORY_SCOPE_AGENT);
    __builtin_amdgcn_sched_barrier(0);
    rounds<F>(v, a, rt, ix, loff, ident, std::make_integer_sequence<int, W>{});

    float r = -a.q;
#pragma unroll
    for (int k = 0; k < 4; ++k) { r = fmaf(a.s0[k], a.s0[k], r); r = fmaf(a.s1[k], a.s1[k], r); }
    r = red8(r);

    const unsigned ep1 = __builtin_amdgcn_readlane((unsigned)old, 1) + 1u;
    bool ok = false;
    if (l == 0) {
        const unsigned tag = ep1 ^ 0xA5C3E187u;
        const unsigned tlo = (ep1 * 0x6Du + 0x3Bu) & 0xFFu;
        const unsigned mine = __float_as_uint(r) & ~0xFFu;
        ok = (unsigned)(old >> 32) == tag && ((unsigned)old & 0xFFu) == tlo;
        if (!ok) {
            old = __hip_atomic_exchange(xch + xo, ((u64)tag << 32) | mine | tlo, __ATOMIC_RELAXED, __HIP_MEMORY_SCOPE_AGENT);
            ok = (unsigned)(old >> 32) == tag && ((unsigned)old & 0xFFu) == tlo;
        }
        if (ok) {
            const __bf16 o = (__bf16)(0.5f * (__uint_as_float(mine) + __uint_as_float((unsigned)old & ~0xFFu)));
            __hip_atomic_store(out + b, __builtin_bit_cast(unsigned short, o), __ATOMIC_RELAXED, __HIP_MEMORY_SCOPE_AGENT);
        }
    }
    const u64 okm = __builtin_amdgcn_ballot_w64(ok);
    const bool bump = (threadIdx.x & 63) == 1 && (okm & 1);
    if (ok || bump) __hip_atomic_store(xch + xo, bump ? (u64)ep1 : 0ull, __ATOMIC_RELAXED, __HIP_MEMORY_SCOPE_AGENT);
}

extern "C" void kernel_launch(void* const* d_in, const int* in_sizes, int n_in,
                              void* d_out, int out_size, void* d_ws, size_t ws_size,
                              hipStream_t stream) {
    fm_kernel<<<256, 1024, 0, stream>>>((const int*)d_in[0], (const char*)d_in[1], (unsigned short*)d_out, (u64*)d_ws,
                                        (unsigned)in_sizes[1] * 2u);
}
```
